```python
import torch

def _softmax(x, axis=-1):
    return torch.softmax(x, dim=axis)
def _gelu(x):
    return torch.nn.functional.gelu(x, approximate="tanh")
def _silu(x):
    return torch.nn.functional.silu(x)
def _sigmoid(x):
    return torch.sigmoid(x)
def _layernorm(x, axis=-1, eps=1e-5):
    m = x.mean(dim=axis, keepdim=True)
    v = x.var(dim=axis, keepdim=True, unbiased=False)
    return (x - m) / torch.sqrt(v + eps)
def _scatter_add(tgt, idx, src, axis=0):
    return tgt.clone().index_add_(axis, idx, src)
def _segment_sum(src, seg, n):
    out = torch.zeros((n,) + src.shape[1:], dtype=src.dtype)
    return out.index_add_(0, seg, src)
def _pad(x, pw):
    flat = tuple(v for pair in reversed(pw) for v in pair)
    return torch.nn.functional.pad(x, flat)

kernel_name = 'standalone__gated_linear_recurrence__prompt'

def setup_inputs():

    _w = (lambda a: (lambda t: t.to(torch.bfloat16) if t.is_floating_point() else t)(torch.as_tensor(a)))
    B, H, T, D = 1, 32, 2048, 128
    return {
        "q": _w((lambda *s: torch.randn(*s, dtype=torch.bfloat16) * 0.05)(B, H, T, D)),
        "k": _w((lambda *s: torch.randn(*s, dtype=torch.bfloat16) * 0.05)(B, H, T, D)),
        "v": _w((lambda *s: torch.randn(*s, dtype=torch.bfloat16) * 0.05)(B, H, T, D)),
        "g": _w((lambda *s: torch.randn(*s, dtype=torch.bfloat16) * 0.8)(B, H, T, D)),
    }

def reference(q, k, v, g):
    B, H, T, D = q.shape
    scale = D ** -0.5
    a = _sigmoid(g)
    S = k[:, :, 0, :, None] * v[:, :, 0, None, :] * 0.0
    outs = []
    for t in range(T):
        S = a[:, :, t, :, None] * S + k[:, :, t, :, None] * v[:, :, t, None, :]
        ot = torch.einsum("bhd,bhde->bhe", q[:, :, t, :], S) * scale
        outs.append(ot)
    return torch.stack(outs, 2)
```

```cpp
#include <hip/hip_runtime.h>
#include <hip/hip_bf16.h>
typedef unsigned short u16;
__device__ __forceinline__ float bf2f(u16 x) { return __uint_as_float((unsigned)x << 16); }
__device__ __forceinline__ u16 f2bf(float x) { __hip_bfloat16 b = __float2bfloat16(x); return *(u16*)&b; }

__global__ __launch_bounds__(128) void naive_kernel(const u16* q, const u16* k, const u16* v, const u16* g, u16* out, int T) {
    const int h = blockIdx.x >> 2, es = blockIdx.x & 3;
    const int tid = threadIdx.x, dq = tid >> 5, e = (tid & 31) + 32 * es;
    __shared__ float sq[128], sk[128], sa[128], so[4][32];
    float S[32];
#pragma unroll
    for (int i = 0; i < 32; ++i) S[i] = 0.f;
    const float scale = 0.08838834764831845f;
    for (int t = 0; t < T; ++t) {
        size_t base = ((size_t)h * T + t) * 128;
        __syncthreads();
        sq[tid] = bf2f(q[base + tid]);
        sk[tid] = bf2f(k[base + tid]);
        float gg = bf2f(g[base + tid]);
        sa[tid] = 1.f / (1.f + __expf(-gg));
        float vv = bf2f(v[base + e]);
        __syncthreads();
        float o = 0.f;
#pragma unroll
        for (int i = 0; i < 32; ++i) {
            int d = dq * 32 + i;
            S[i] = sa[d] * S[i] + sk[d] * vv;
            o += sq[d] * S[i];
        }
        so[dq][tid & 31] = o;
        __syncthreads();
        if (tid < 32) out[base + e] = f2bf((so[0][tid] + so[1][tid] + so[2][tid] + so[3][tid]) * scale);
    }
}

extern "C" void kernel_launch(void* const* d_in, const int* in_sizes, int n_in,
                              void* d_out, int out_size, void* d_ws, size_t ws_size,
                              hipStream_t stream) {
    naive_kernel<<<32 * 4, 128, 0, stream>>>((const u16*)d_in[0], (const u16*)d_in[1], (const u16*)d_in[2], (const u16*)d_in[3], (u16*)d_out, 2048);
}
```

```cpp
#include <hip/hip_runtime.h>
#include <stdint.h>

typedef unsigned int u32;
typedef unsigned long long u64;
typedef u32 u32x2 __attribute__((ext_vector_type(2)));
typedef u32 u32x4 __attribute__((ext_vector_type(4)));
typedef float f32x2 __attribute__((ext_vector_type(2)));
typedef float f32x4 __attribute__((ext_vector_type(4)));
typedef float f32x16 __attribute__((ext_vector_type(16)));
typedef __bf16 bf16x2 __attribute__((ext_vector_type(2)));
typedef __bf16 bf16x8 __attribute__((ext_vector_type(8)));
typedef short s16x4 __attribute__((ext_vector_type(4)));
#define LAS __attribute__((address_space(3)))
#define GAS __attribute__((address_space(1)))
#define DI __device__ __forceinline__
#define RLX_AGENT __ATOMIC_RELAXED, __HIP_MEMORY_SCOPE_AGENT

#define NT_LOADS 1

constexpr int TT = 2048;
constexpr u32 L_KT = 0, L_V = 65536, L_X0 = 131072, L_AT = 147456, L_DEC = 153600, L_W = 155648, L_MISC = 159232, L_TOTAL = 159744;
constexpr size_t WS_D = 16384, WS_S = 1u << 20, WS_STAMP = 32u << 20;
constexpr float C0 = 0x1p-56f, PLIM = 0x1p56f;
constexpr float NLOG2E = -1.4426950408889634f, SCALE = 0.08838834764831845f;
constexpr u32 SPIN_CAP = 1u << 18;

#define BAR() asm volatile("s_waitcnt lgkmcnt(0)\n\ts_barrier" ::: "memory")
#define SCHED() __builtin_amdgcn_sched_barrier(0)
#define PIN(x) asm volatile("" : "+v"(x))
#define MFMA(a, b, c) __builtin_amdgcn_mfma_f32_32x32x16_bf16((a), (b), (c), 0, 0, 0)

#define STAMP(i) do {} while (0)
#define STAMP_FLUSH() do {} while (0)

DI f32x2 unpk(u32 x) { return (f32x2){__uint_as_float(x << 16), __uint_as_float(x & 0xffff0000u)}; }
DI u32 pk(float a, float b) { return __builtin_bit_cast(u32, __builtin_convertvector((f32x2){a, b}, bf16x2)); }
DI u32 ldrow(__amdgpu_buffer_rsrc_t rs, u32 voff, int t) {
    return __builtin_amdgcn_raw_buffer_load_b32(rs, voff + (t & 15) * 256, (t >> 4) * 4096, NT_LOADS ? 2 : 0);
}
DI bf16x8 trfrag(LAS char* a0, LAS char* a1) {
    s16x4 lo = __builtin_amdgcn_ds_read_tr16_b64_v4i16((LAS s16x4*)a0);
    s16x4 hi = __builtin_amdgcn_ds_read_tr16_b64_v4i16((LAS s16x4*)a1);
    return __builtin_bit_cast(bf16x8, __builtin_shufflevector(lo, hi, 0, 1, 2, 3, 4, 5, 6, 7));
}
DI bf16x8 packstep(const f32x16& x, int s) {
    u32x4 p = {pk(x[8 * s], x[8 * s + 1]), pk(x[8 * s + 2], x[8 * s + 3]), pk(x[8 * s + 4], x[8 * s + 5]), pk(x[8 * s + 6], x[8 * s + 7])};
    return __builtin_bit_cast(bf16x8, p);
}
DI u64 mix_tag(u64 n) { return n * 6364136223846793005ull + 1442695040888963407ull; }

__global__ __launch_bounds__(512, 2) void gla_kernel(const u32* __restrict__ q_, const u32* __restrict__ k_, const u32* __restrict__ v_,
                                                     const u32* __restrict__ g_, u32* __restrict__ out_, char* __restrict__ ws_) {
    __shared__ __attribute__((aligned(16))) char smem_[L_TOTAL];
    LAS char* sm = (LAS char*)smem_;
    const u32 lane = threadIdx.x & 63;
    const int wave = __builtin_amdgcn_readfirstlane(threadIdx.x >> 6);
    const int b = blockIdx.x;
    const int head = (b & 7) * 4 + ((b >> 3) & 3), j = b >> 5;
    const size_t segrow = (size_t)head * TT + j * 256;
    GAS char* ws = (GAS char*)ws_;
    GAS u64* flags = (GAS u64*)(ws + head * 256);
    STAMP(0);

    if (wave < 4) {
        const int c = wave;
        const size_t off = (segrow + c * 64) * 64;
        const __amdgpu_buffer_rsrc_t gp = __builtin_amdgcn_make_buffer_rsrc((void*)(g_ + off), 0, 16384, 0x00020000);
        const __amdgpu_buffer_rsrc_t kp = __builtin_amdgcn_make_buffer_rsrc((void*)(k_ + off), 0, 16384, 0x00020000);
        const __amdgpu_buffer_rsrc_t qp = __builtin_amdgcn_make_buffer_rsrc((void*)(q_ + off), 0, 16384, 0x00020000);
        const u32 l4 = lane * 4;
        u32 raw[64];
#pragma unroll
        for (int t = 0; t < 64; ++t) { raw[t] = ldrow(gp, l4, t); SCHED(); }
        f32x2 P[64];
        f32x2 p = {C0, C0};
#pragma unroll
        for (int t = 0; t < 64; ++t) {
            f32x2 e = unpk(raw[t]) * NLOG2E;
            e.x = __builtin_amdgcn_exp2f(e.x);
            e.y = __builtin_amdgcn_exp2f(e.y);
            p = __builtin_elementwise_fma(p, e, p);
            PIN(p);
            P[t] = p;
            raw[t] = ldrow(kp, l4, t);
            if ((t & 3) == 3) SCHED();
        }
        {
            f32x2 dec = {C0 * __builtin_amdgcn_rcpf(p.x), C0 * __builtin_amdgcn_rcpf(p.y)};
            *(LAS f32x2*)(sm + L_DEC + c * 512 + lane * 8) = dec;
            bool bad = !(p.x <= PLIM && p.y <= PLIM);
            u32 anybad = __ballot(bad) != 0;
            if (lane == 0) *(LAS u32*)(sm + L_MISC + c * 4) = anybad;
        }
        STAMP(1);
        BAR();
        const u32 fsw = ((lane & 1) << 2) | ((lane >> 1) & 3);
        LAS char* wrow = sm + lane * 256;
#pragma unroll
        for (int m = 0; m < 8; ++m) {
            u32x4 a, bb;
#pragma unroll
            for (int i = 0; i < 4; ++i) {
                const int t = 8 * m + 2 * i;
                f32x2 x0 = unpk(raw[t]) * P[t], x1 = unpk(raw[t + 1]) * P[t + 1];
                a[i] = pk(x0.x, x1.x);
                bb[i] = pk(x0.y, x1.y);
            }
            LAS char* dst = wrow + L_KT + c * 16384 + ((m ^ fsw) << 4);
            *(LAS u32x4*)dst = a;
            *(LAS u32x4*)(dst + 128) = bb;
            SCHED();
        }
        STAMP(2);
        BAR();
        const u64 tag = *(LAS u64*)(sm + L_MISC + 32);
        const u32 tag32 = (u32)tag;
        u32x4 dg[7];
        const __amdgpu_buffer_rsrc_t drs = __builtin_amdgcn_make_buffer_rsrc((void*)(ws_ + WS_D + (size_t)head * 8192), 0, 8192, 0x00020000);
        const u32 dvo = (lane & 31) * 32 + (lane >> 5) * 16;
        if (c == 3) {
#pragma unroll
            for (int n = 1; n < 7; ++n)
                if (n < j) dg[n] = __builtin_amdgcn_raw_buffer_load_b128(drs, dvo, (j - n) * 1024, 16);
        }
#pragma unroll
        for (int t = 0; t < 64; ++t) {
            P[t].x = __builtin_amdgcn_rcpf(P[t].x);
            P[t].y = __builtin_amdgcn_rcpf(P[t].y);
            P[t] *= SCALE;
            PIN(P[t]);
        }
        f32x2 F = {1.f, 1.f};
#pragma unroll
        for (int cc = 0; cc < 3; ++cc)
            if (cc < c) F *= *(LAS f32x2*)(sm + L_DEC + cc * 512 + lane * 8);
        for (int i = 0; i <= c; ++i) BAR();
#pragma unroll
        for (int t = 0; t < 64; ++t) { raw[t] = ldrow(qp, l4, t); SCHED(); }
        STAMP(3);
        if (c == 3) {
            f32x2 wv = {1.f, 1.f};
            *(LAS f32x2*)(sm + L_W + (lane & 31) * 16 + (lane >> 5) * 8) = wv;
#pragma unroll
            for (int n = 1; n < 7; ++n)
                if (n < j) {
                    u32x4 x = dg[n];
                    for (u32 spins = 0; !__all(x[1] == tag32 && x[3] == tag32) && spins < SPIN_CAP; ++spins) {
                        __builtin_amdgcn_s_sleep(2);
                        x = __builtin_amdgcn_raw_buffer_load_b128(drs, dvo, (j - n) * 1024, 16);
                    }
                    wv *= (f32x2){__uint_as_float(x[0]), __uint_as_float(x[2])};
                    *(LAS f32x2*)(sm + L_W + n * 512 + (lane & 31) * 16 + (lane >> 5) * 8) = wv;
                }
        }
        const int npre = (3 - c) + (c == 0 ? 0 : 2 * c - 1);
        for (int i = 0; i < npre; ++i) BAR();
        u32 qa[2][32], qb[2][32];
#pragma unroll
        for (int i = 0; i < 32; ++i) {
            const int t = 2 * i;
            f32x2 x0 = unpk(raw[t]) * P[t], x1 = unpk(raw[t + 1]) * P[t + 1];
            qa[0][i] = pk(x0.x, x1.x);
            qa[1][i] = pk(x0.y, x1.y);
            x0 *= F;
            x1 *= F;
            qb[0][i] = pk(x0.x, x1.x);
            qb[1][i] = pk(x0.y, x1.y);
            PIN(qa[0][i]); PIN(qa[1][i]); PIN(qb[0][i]); PIN(qb[1][i]);
            if ((i & 3) == 3) SCHED();
        }
        STAMP(4);
        if (c > 0) BAR();
        {
            const u32 slotA = c == 0 ? L_X0 : L_KT + (c - 1) * 16384;
#pragma unroll
            for (int m = 0; m < 8; ++m) {
                LAS char* dst = wrow + slotA + ((m ^ fsw) << 4);
                *(LAS u32x4*)dst = (u32x4){qa[0][4 * m], qa[0][4 * m + 1], qa[0][4 * m + 2], qa[0][4 * m + 3]};
                *(LAS u32x4*)(dst + 128) = (u32x4){qa[1][4 * m], qa[1][4 * m + 1], qa[1][4 * m + 2], qa[1][4 * m + 3]};
            }
            if (c > 0) {
                const u32 slotB = L_V + (c - 1) * 16384;
#pragma unroll
                for (int m = 0; m < 8; ++m) {
                    LAS char* dst = wrow + slotB + ((m ^ fsw) << 4);
                    *(LAS u32x4*)dst = (u32x4){qb[0][4 * m], qb[0][4 * m + 1], qb[0][4 * m + 2], qb[0][4 * m + 3]};
                    *(LAS u32x4*)(dst + 128) = (u32x4){qb[1][4 * m], qb[1][4 * m + 1], qb[1][4 * m + 2], qb[1][4 * m + 3]};
                }
            }
        }
        BAR();
        int rem = 6 - 2 * c;
        if (c == 0) { BAR(); BAR(); rem -= 2; }
        STAMP(5);
        u32x4 pl[7][8];
        const __amdgpu_buffer_rsrc_t srs = __builtin_amdgcn_make_buffer_rsrc((void*)(ws_ + WS_S + (size_t)head * 8 * 32768), 0, 8 * 32768, 0x00020000);
        if (j > 0) {
            for (u32 spins = 0; spins < SPIN_CAP; ++spins) {
                u64 f = tag;
                if (lane < (u32)j) f = __hip_atomic_load(flags + lane, RLX_AGENT);
                if (__all(f == tag)) break;
                __builtin_amdgcn_s_sleep(1);
            }
            __builtin_amdgcn_fence(__ATOMIC_ACQUIRE, "wavefront");
            STAMP(6);
            if (j == 7 && c == 0 && lane == 0) __hip_atomic_store(flags + 16, tag, RLX_AGENT);
#pragma unroll
            for (int n = 0; n < 5; ++n)
                if (n < j) {
#pragma unroll
                    for (int f = 0; f < 8; ++f)
                        pl[n][f] = __builtin_amdgcn_raw_buffer_load_b128(srs, lane * 16 + (f & 3) * 1024, (j - 1 - n) * 32768 + c * 8192 + (f >> 2) * 4096, 16);
                }
        }
        for (int i = 0; i < rem; ++i) BAR();
        STAMP(7);
        if (j > 0) {
            f32x16 acc[4];
#pragma unroll
            for (int mt = 0; mt < 4; ++mt)
#pragma unroll
                for (int i = 0; i < 16; ++i) acc[mt][i] = 0.f;
            const u32 r = lane & 31, h = lane >> 5;
            const bool act = h == ((r >> 2) & 1);
            const u32 sel = act ? ((r >> 4) * 4 + ((r >> 3) & 1) * 2 + ((r >> 1) & 1)) : 99u;
            const u32 hm = (r & 1) ? 0xffff0000u : 0x0000ffffu;
#pragma unroll
            for (int n = 0; n < 7; ++n) {
                if (n < j) {
                    const f32x4 w4 = *(LAS f32x4*)(sm + L_W + n * 512 + r * 16);
#pragma unroll
                    for (int mt = 0; mt < 4; ++mt) {
                        const u32 wp = pk(w4[mt], w4[mt]) & hm;
#pragma unroll
                        for (int s = 0; s < 2; ++s) {
                            u32x4 dgl = {sel == 4 * s + 0 ? wp : 0u, sel == 4 * s + 1 ? wp : 0u, sel == 4 * s + 2 ? wp : 0u, sel == 4 * s + 3 ? wp : 0u};
                            acc[mt] = MFMA(__builtin_bit_cast(bf16x8, dgl), __builtin_bit_cast(bf16x8, pl[n][2 * mt + s]), acc[mt]);
                        }
                    }
                }
                if (n < 2 && n + 5 < j) {
#pragma unroll
                    for (int f = 0; f < 8; ++f)
                        pl[n + 5][f] = __builtin_amdgcn_raw_buffer_load_b128(srs, lane * 16 + (f & 3) * 1024, (j - 6 - n) * 32768 + c * 8192 + (f >> 2) * 4096, 16);
                }
            }
#pragma unroll
            for (int mt = 0; mt < 4; ++mt)
#pragma unroll
                for (int s = 0; s < 2; ++s) *(LAS bf16x8*)(sm + L_KT + c * 8192 + (2 * mt + s) * 1024 + lane * 16) = packstep(acc[mt], s);
        }
        STAMP(8);
        BAR();
        STAMP_FLUSH();
    } else {
        const int w = wave - 4;
        const u32 r = lane & 31, h = lane >> 5, blk = (lane >> 4) & 1, qq = (lane & 15) >> 2, pp = lane & 3;
        u64 tag;
        {
            u64 nn = __hip_atomic_load(flags + 16, RLX_AGENT);
            u32 lo = __builtin_amdgcn_readfirstlane((u32)nn), hi = __builtin_amdgcn_readfirstlane((u32)(nn >> 32));
            tag = mix_tag(((u64)hi << 32) | lo);
            if (w == 0 && lane == 0) { *(LAS u64*)(sm + L_MISC + 32) = tag; *(LAS u32*)(sm + L_MISC + 16) = 0u; }
        }
        BAR();
        STAMP(1);
        if (j < 7) {
            LAS float* dp = (LAS float*)(sm + L_DEC) + 32 * w + r;
            float dd = (dp[0] * dp[128]) * (dp[256] * dp[384]);
            if (lane < 32) __hip_atomic_store((GAS u64*)(ws + WS_D + (size_t)(head * 8 + j) * 1024) + r * 4 + w, ((u64)(u32)tag << 32) | __float_as_uint(dd), RLX_AGENT);
        }
        {
            const u32 voff = (lane >> 4) * 256 + (((lane & 15) ^ ((lane >> 4) << 2)) << 4);
            const char* vb = (const char*)v_ + (segrow + w * 16) * 256;
            const u32 ldsbase = (u32)(uintptr_t)(sm + L_V + w * 4096);
#pragma unroll
            for (int c = 0; c < 4; ++c) {
#define NTS " nt"
                asm volatile("s_mov_b32 m0, %2\n\ts_nop 0\n\t"
                             "global_load_lds_dwordx4 %0, %1" NTS "\n\t"
                             "global_load_lds_dwordx4 %0, %1 offset:1024" NTS "\n\t"
                             "global_load_lds_dwordx4 %0, %1 offset:2048" NTS "\n\t"
                             "global_load_lds_dwordx4 %0, %1 offset:3072" NTS
                             :: "v"(voff), "s"(vb + c * 16384), "s"(ldsbase + c * 16384) : "memory");
            }
        }
        const u32 fr = ((r & 2) << 1) | ((r >> 2) & 3);
        LAS char* krow[4];
#pragma unroll
        for (int ks = 0; ks < 4; ++ks) krow[ks] = sm + L_KT + r * 128 + (((2 * ks + h) ^ fr) << 4);
        const u32 eq = 4 * blk + (((pp & 1) << 1) | (pp >> 1));
        const u32 vcol = ((4 * (w ^ qq) + (eq >> 1)) << 4) + 8 * (eq & 1);
        LAS char* vnat = sm + L_V + (8 * h + qq) * 256 + vcol;
        LAS char* vper = sm + L_V + (4 * h + qq) * 256 + vcol;
        u32 tro[2][2];
#pragma unroll
        for (int tl = 0; tl < 2; ++tl)
#pragma unroll
            for (int jj = 0; jj < 2; ++jj)
                tro[tl][jj] = (8 * jj + 4 * h + qq) * 128 + (((4 * (tl ^ (qq >> 1))) + 2 * (blk ^ jj) + ((pp >> 1) ^ h)) << 4) + 8 * (pp & 1);
        LAS char* decp = sm + L_DEC + h * 16;
        BAR();
        STAMP(2);
        f32x16 W[4];
#pragma unroll
        for (int mt = 0; mt < 4; ++mt)
#pragma unroll
            for (int i = 0; i < 16; ++i) W[mt][i] = 0.f;
        bf16x8 Sf[5][8];
#pragma unroll
        for (int c = 0; c < 4; ++c) {
            if (c == 0) asm volatile("s_waitcnt vmcnt(12)" ::: "memory");
            if (c == 1) asm volatile("s_waitcnt vmcnt(8)" ::: "memory");
            if (c == 2) asm volatile("s_waitcnt vmcnt(4)" ::: "memory");
            if (c == 3) asm volatile("s_waitcnt vmcnt(0)" ::: "memory");
            BAR();
            STAMP(3 + c);
            bf16x8 Vf[4];
#pragma unroll
            for (int ks = 0; ks < 4; ++ks) Vf[ks] = trfrag(vnat + c * 16384 + ks * 4096, vnat + c * 16384 + ks * 4096 + 1024);
#pragma unroll
            for (int mt = 0; mt < 4; ++mt) {
#pragma unroll
                for (int ks = 0; ks < 4; ++ks) {
                    bf16x8 a = *(LAS bf16x8*)(krow[ks] + c * 16384 + mt * 4096);
                    W[mt] = MFMA(a, Vf[ks], W[mt]);
                }
            }
#pragma unroll
            for (int mt = 0; mt < 4; ++mt) {
#pragma unroll
                for (int gq = 0; gq < 4; ++gq) {
                    f32x4 dv = *(LAS f32x4*)(decp + c * 512 + mt * 128 + gq * 32);
#pragma unroll
                    for (int i = 0; i < 4; ++i) W[mt][4 * gq + i] *= dv[i];
                }
                Sf[c + 1][2 * mt] = packstep(W[mt], 0);
                Sf[c + 1][2 * mt + 1] = packstep(W[mt], 1);
                PIN(Sf[c + 1][2 * mt]); PIN(Sf[c + 1][2 * mt + 1]);
                if (c < 3) PIN(W[mt]);
            }
        }
        STAMP(7);
        if (j < 7) {
            const __amdgpu_buffer_rsrc_t prs = __builtin_amdgcn_make_buffer_rsrc((void*)(ws_ + WS_S + (size_t)(head * 8 + j) * 32768 + w * 8192), 0, 8192, 0x00020000);
#pragma unroll
            for (int f = 0; f < 8; ++f)
                __builtin_amdgcn_raw_buffer_store_b128(__builtin_bit_cast(u32x4, Sf[4][f]), prs, lane * 16 + (f & 3) * 1024, (f >> 2) * 4096, 16);
            asm volatile("s_waitcnt vmcnt(0)" ::: "memory");
            if (lane == 0) {
                u32 old = __hip_atomic_fetch_add((LAS u32*)(sm + L_MISC + 16), 1u, __ATOMIC_RELAXED, __HIP_MEMORY_SCOPE_WORKGROUP);
                if (old == 3) __hip_atomic_store(flags + j, tag, RLX_AGENT);
            }
        }
        STAMP(8);
        BAR();
        STAMP(9);
        f32x16 o[4][2];
        const u32 sg = w == 2 ? 1 : 0, tu = w == 0 ? 0 : 1;
        const u32 ka0 = sg ? tro[1][0] : tro[0][0], ka1 = sg ? tro[1][1] : tro[0][1];
        const u32 qa0 = tu ? tro[1][0] : tro[0][0], qa1 = tu ? tro[1][1] : tro[0][1];
        const int thr = (w == 1 ? 64 : (int)r) - 4 * (int)h;
#pragma unroll
        for (int c = 0; c < 4; ++c) {
            const u32 qslot = c == 0 ? L_X0 : L_KT + (c - 1) * 16384;
            if (w < 3) {
                f32x16 X;
#pragma unroll
                for (int i = 0; i < 16; ++i) X[i] = 0.f;
#pragma unroll
                for (int kk = 0; kk < 8; ++kk) {
                    bf16x8 a = trfrag(sm + L_KT + c * 16384 + kk * 2048 + ka0, sm + L_KT + c * 16384 + kk * 2048 + ka1);
                    bf16x8 bq = trfrag(sm + qslot + kk * 2048 + qa0, sm + qslot + kk * 2048 + qa1);
                    X = MFMA(a, bq, X);
                }
#pragma unroll
                for (int i = 0; i < 16; ++i) X[i] = ((i & 3) + 8 * (i >> 2)) <= thr ? X[i] : 0.f;
                *(LAS bf16x8*)(sm + L_AT + w * 2048 + lane * 16) = packstep(X, 0);
                *(LAS bf16x8*)(sm + L_AT + w * 2048 + 1024 + lane * 16) = packstep(X, 1);
            }
#pragma unroll
            for (int tl = 0; tl < 2; ++tl)
#pragma unroll
                for (int i = 0; i < 16; ++i) o[c][tl][i] = 0.f;
            if (c > 0) {
#pragma unroll
                for (int kk = 0; kk < 8; ++kk)
#pragma unroll
                    for (int tl = 0; tl < 2; ++tl)
                        o[c][tl] = MFMA(Sf[c][kk], trfrag(sm + qslot + kk * 2048 + tro[tl][0], sm + qslot + kk * 2048 + tro[tl][1]), o[c][tl]);
            }
            bf16x8 Vp[4];
#pragma unroll
            for (int ks = 0; ks < 4; ++ks) Vp[ks] = trfrag(vper + c * 16384 + ks * 4096, vper + c * 16384 + ks * 4096 + 2048);
            PIN(o[c][0]); PIN(o[c][1]);
            BAR();
            STAMP(10 + 2 * c);
            LAS char* at = sm + L_AT + lane * 16;
            o[c][0] = MFMA(Vp[0], *(LAS bf16x8*)(at), o[c][0]);
            o[c][1] = MFMA(Vp[0], *(LAS bf16x8*)(at + 2048), o[c][1]);
            o[c][0] = MFMA(Vp[1], *(LAS bf16x8*)(at + 1024), o[c][0]);
            o[c][1] = MFMA(Vp[1], *(LAS bf16x8*)(at + 3072), o[c][1]);
            o[c][1] = MFMA(Vp[2], *(LAS bf16x8*)(at + 4096), o[c][1]);
            o[c][1] = MFMA(Vp[3], *(LAS bf16x8*)(at + 5120), o[c][1]);
            PIN(o[c][0]); PIN(o[c][1]);
            if (c < 3) { BAR(); STAMP(11 + 2 * c); }
        }
        bf16x8 Sin[8];
        if (j > 0) {
#pragma unroll
            for (int kk = 0; kk < 8; ++kk) Sin[kk] = *(LAS bf16x8*)(sm + L_KT + w * 8192 + kk * 1024 + lane * 16);
        }
        GAS char* op = (GAS char*)out_ + (segrow + r) * 256 + w * 64 + h * 16;
#pragma unroll
        for (int c = 0; c < 4; ++c) {
            const u32 qslot = c == 0 ? L_X0 : L_V + (c - 1) * 16384;
#pragma unroll
            for (int tl = 0; tl < 2; ++tl) {
                if (j > 0) {
#pragma unroll
                    for (int kk = 0; kk < 8; ++kk)
                        o[c][tl] = MFMA(Sin[kk], trfrag(sm + qslot + kk * 2048 + tro[tl][0], sm + qslot + kk * 2048 + tro[tl][1]), o[c][tl]);
                }
                *(GAS bf16x8*)(op + (c * 64 + tl * 32) * 256) = packstep(o[c][tl], 0);
                *(GAS bf16x8*)(op + (c * 64 + tl * 32) * 256 + 32) = packstep(o[c][tl], 1);
            }
        }
        STAMP(18);
    }
}

extern "C" void kernel_launch(void* const* d_in, const int* in_sizes, int n_in, void* d_out, int out_size, void* d_ws, size_t ws_size,
                              hipStream_t stream) {
    gla_kernel<<<256, 512, 0, stream>>>((const u32*)d_in[0], (const u32*)d_in[1], (const u32*)d_in[2], (const u32*)d_in[3], (u32*)d_out, (char*)d_ws);
}
```

```cpp
#define PROBE 0
#include <hip/hip_runtime.h>
#include <stdint.h>

typedef unsigned int u32;
typedef unsigned long long u64;
typedef u32 u32x2 __attribute__((ext_vector_type(2)));
typedef u32 u32x4 __attribute__((ext_vector_type(4)));
typedef float f32x2 __attribute__((ext_vector_type(2)));
typedef float f32x4 __attribute__((ext_vector_type(4)));
typedef float f32x16 __attribute__((ext_vector_type(16)));
typedef __bf16 bf16x2 __attribute__((ext_vector_type(2)));
typedef __bf16 bf16x8 __attribute__((ext_vector_type(8)));
typedef short s16x4 __attribute__((ext_vector_type(4)));
#define LAS __attribute__((address_space(3)))
#define GAS __attribute__((address_space(1)))
#define DI __device__ __forceinline__
#define RLX_AGENT __ATOMIC_RELAXED, __HIP_MEMORY_SCOPE_AGENT

#define NT_LOADS 1

constexpr int TT = 2048;
constexpr u32 L_KT = 0, L_V = 65536, L_X0 = 131072, L_AT = 147456, L_DEC = 153600, L_W = 155648, L_MISC = 159232, L_TOTAL = 159744;
constexpr size_t WS_D = 16384, WS_S = 1u << 20, WS_STAMP = 32u << 20;
constexpr float C0 = 0x1p-56f, PLIM = 0x1p56f;
constexpr float NLOG2E = -1.4426950408889634f, SCALE = 0.08838834764831845f;
constexpr u32 SPIN_CAP = 1u << 18;

#define BAR() asm volatile("s_waitcnt lgkmcnt(0)\n\ts_barrier" ::: "memory")
#define SCHED() __builtin_amdgcn_sched_barrier(0)
#define PIN(x) asm volatile("" : "+v"(x))
#define MFMA(a, b, c) __builtin_amdgcn_mfma_f32_32x32x16_bf16((a), (b), (c), 0, 0, 0)

#define STAMP(i) do { if ((i) == PROBE || (i) == 0) { u64 t_; asm volatile("s_memrealtime %0\n\ts_waitcnt lgkmcnt(0)" : "=s"(t_) :: "memory"); if ((i) == 0) pt0 = t_; if ((i) == PROBE) pt1 = t_; } } while (0)
#define STAMP_FLUSH() do {} while (0)

DI f32x2 unpk(u32 x) { return (f32x2){__uint_as_float(x << 16), __uint_as_float(x & 0xffff0000u)}; }
DI u32 pk(float a, float b) { return __builtin_bit_cast(u32, __builtin_convertvector((f32x2){a, b}, bf16x2)); }
DI u32 ldrow(__amdgpu_buffer_rsrc_t rs, u32 voff, int t) {
    return __builtin_amdgcn_raw_buffer_load_b32(rs, voff + (t & 15) * 256, (t >> 4) * 4096, NT_LOADS ? 2 : 0);
}
DI bf16x8 trfrag(LAS char* a0, LAS char* a1) {
    s16x4 lo = __builtin_amdgcn_ds_read_tr16_b64_v4i16((LAS s16x4*)a0);
    s16x4 hi = __builtin_amdgcn_ds_read_tr16_b64_v4i16((LAS s16x4*)a1);
    return __builtin_bit_cast(bf16x8, __builtin_shufflevector(lo, hi, 0, 1, 2, 3, 4, 5, 6, 7));
}
DI bf16x8 packstep(const f32x16& x, int s) {
    u32x4 p = {pk(x[8 * s], x[8 * s + 1]), pk(x[8 * s + 2], x[8 * s + 3]), pk(x[8 * s + 4], x[8 * s + 5]), pk(x[8 * s + 6], x[8 * s + 7])};
    return __builtin_bit_cast(bf16x8, p);
}
DI u64 mix_tag(u64 n) { return n * 6364136223846793005ull + 1442695040888963407ull; }

__global__ __launch_bounds__(512, 2) void gla_kernel(const u32* __restrict__ q_, const u32* __restrict__ k_, const u32* __restrict__ v_,
                                                     const u32* __restrict__ g_, u32* __restrict__ out_, char* __restrict__ ws_) {
    __shared__ __attribute__((aligned(16))) char smem_[L_TOTAL];
    LAS char* sm = (LAS char*)smem_;
    const u32 lane = threadIdx.x & 63;
    const int wave = __builtin_amdgcn_readfirstlane(threadIdx.x >> 6);
    const int b = blockIdx.x;
    const int head = (b & 7) * 4 + ((b >> 3) & 3), j = b >> 5;
    const size_t segrow = (size_t)head * TT + j * 256;
    GAS char* ws = (GAS char*)ws_;
    GAS u64* flags = (GAS u64*)(ws + head * 256);
    u64 pt0 = 0, pt1 = 0;
    STAMP(0);

    if (wave < 4) {
        const int c = wave;
        const size_t off = (segrow + c * 64) * 64;
        const __amdgpu_buffer_rsrc_t gp = __builtin_amdgcn_make_buffer_rsrc((void*)(g_ + off), 0, 16384, 0x00020000);
        const __amdgpu_buffer_rsrc_t kp = __builtin_amdgcn_make_buffer_rsrc((void*)(k_ + off), 0, 16384, 0x00020000);
        const __amdgpu_buffer_rsrc_t qp = __builtin_amdgcn_make_buffer_rsrc((void*)(q_ + off), 0, 16384, 0x00020000);
        const u32 l4 = lane * 4;
        u32 raw[64];
#pragma unroll
        for (int t = 0; t < 64; ++t) { raw[t] = ldrow(gp, l4, t); SCHED(); }
        f32x2 P[64];
        f32x2 p = {C0, C0};
#pragma unroll
        for (int t = 0; t < 64; ++t) {
            f32x2 e = unpk(raw[t]) * NLOG2E;
            e.x = __builtin_amdgcn_exp2f(e.x);
            e.y = __builtin_amdgcn_exp2f(e.y);
            p = __builtin_elementwise_fma(p, e, p);
            PIN(p);
            P[t] = p;
            raw[t] = ldrow(kp, l4, t);
            if ((t & 3) == 3) SCHED();
        }
        {
            f32x2 dec = {C0 * __builtin_amdgcn_rcpf(p.x), C0 * __builtin_amdgcn_rcpf(p.y)};
            *(LAS f32x2*)(sm + L_DEC + c * 512 + lane * 8) = dec;
            bool bad = !(p.x <= PLIM && p.y <= PLIM);
            u32 anybad = __ballot(bad) != 0;
            if (lane == 0) *(LAS u32*)(sm + L_MISC + c * 4) = anybad;
        }
        STAMP(1);
        BAR();
        const u32 fsw = ((lane & 1) << 2) | ((lane >> 1) & 3);
        LAS char* wrow = sm + lane * 256;
#pragma unroll
        for (int m = 0; m < 8; ++m) {
            u32x4 a, bb;
#pragma unroll
            for (int i = 0; i < 4; ++i) {
                const int t = 8 * m + 2 * i;
                f32x2 x0 = unpk(raw[t]) * P[t], x1 = unpk(raw[t + 1]) * P[t + 1];
                a[i] = pk(x0.x, x1.x);
                bb[i] = pk(x0.y, x1.y);
            }
            LAS char* dst = wrow + L_KT + c * 16384 + ((m ^ fsw) << 4);
            *(LAS u32x4*)dst = a;
            *(LAS u32x4*)(dst + 128) = bb;
            SCHED();
        }
        STAMP(2);
        BAR();
        const u64 tag = *(LAS u64*)(sm + L_MISC + 32);
        const u32 tag32 = (u32)tag;
        u32x4 dg[7];
        const __amdgpu_buffer_rsrc_t drs = __builtin_amdgcn_make_buffer_rsrc((void*)(ws_ + WS_D + (size_t)head * 8192), 0, 8192, 0x00020000);
        const u32 dvo = (lane & 31) * 32 + (lane >> 5) * 16;
        if (c == 3) {
#pragma unroll
            for (int n = 1; n < 7; ++n)
                if (n < j) dg[n] = __builtin_amdgcn_raw_buffer_load_b128(drs, dvo, (j - n) * 1024, 16);
        }
#pragma unroll
        for (int t = 0; t < 64; ++t) {
            P[t].x = __builtin_amdgcn_rcpf(P[t].x);
            P[t].y = __builtin_amdgcn_rcpf(P[t].y);
            P[t] *= SCALE;
            PIN(P[t]);
        }
        f32x2 F = {1.f, 1.f};
#pragma unroll
        for (int cc = 0; cc < 3; ++cc)
            if (cc < c) F *= *(LAS f32x2*)(sm + L_DEC + cc * 512 + lane * 8);
        for (int i = 0; i <= c; ++i) BAR();
#pragma unroll
        for (int t = 0; t < 64; ++t) { raw[t] = ldrow(qp, l4, t); SCHED(); }
        STAMP(3);
        if (c == 3) {
            f32x2 wv = {1.f, 1.f};
            *(LAS f32x2*)(sm + L_W + (lane & 31) * 16 + (lane >> 5) * 8) = wv;
#pragma unroll
            for (int n = 1; n < 7; ++n)
                if (n < j) {
                    u32x4 x = dg[n];
                    for (u32 spins = 0; !__all(x[1] == tag32 && x[3] == tag32) && spins < SPIN_CAP; ++spins) {
                        __builtin_amdgcn_s_sleep(2);
                        x = __builtin_amdgcn_raw_buffer_load_b128(drs, dvo, (j - n) * 1024, 16);
                    }
                    wv *= (f32x2){__uint_as_float(x[0]), __uint_as_float(x[2])};
                    *(LAS f32x2*)(sm + L_W + n * 512 + (lane & 31) * 16 + (lane >> 5) * 8) = wv;
                }
        }
        const int npre = (3 - c) + (c == 0 ? 0 : 2 * c - 1);
        for (int i = 0; i < npre; ++i) BAR();
        u32 qa[2][32], qb[2][32];
#pragma unroll
        for (int i = 0; i < 32; ++i) {
            const int t = 2 * i;
            f32x2 x0 = unpk(raw[t]) * P[t], x1 = unpk(raw[t + 1]) * P[t + 1];
            qa[0][i] = pk(x0.x, x1.x);
            qa[1][i] = pk(x0.y, x1.y);
            x0 *= F;
            x1 *= F;
            qb[0][i] = pk(x0.x, x1.x);
            qb[1][i] = pk(x0.y, x1.y);
            PIN(qa[0][i]); PIN(qa[1][i]); PIN(qb[0][i]); PIN(qb[1][i]);
            if ((i & 3) == 3) SCHED();
        }
        STAMP(4);
        if (c > 0) BAR();
        {
            const u32 slotA = c == 0 ? L_X0 : L_KT + (c - 1) * 16384;
#pragma unroll
            for (int m = 0; m < 8; ++m) {
                LAS char* dst = wrow + slotA + ((m ^ fsw) << 4);
                *(LAS u32x4*)dst = (u32x4){qa[0][4 * m], qa[0][4 * m + 1], qa[0][4 * m + 2], qa[0][4 * m + 3]};
                *(LAS u32x4*)(dst + 128) = (u32x4){qa[1][4 * m], qa[1][4 * m + 1], qa[1][4 * m + 2], qa[1][4 * m + 3]};
            }
            if (c > 0) {
                const u32 slotB = L_V + (c - 1) * 16384;
#pragma unroll
                for (int m = 0; m < 8; ++m) {
                    LAS char* dst = wrow + slotB + ((m ^ fsw) << 4);
                    *(LAS u32x4*)dst = (u32x4){qb[0][4 * m], qb[0][4 * m + 1], qb[0][4 * m + 2], qb[0][4 * m + 3]};
                    *(LAS u32x4*)(dst + 128) = (u32x4){qb[1][4 * m], qb[1][4 * m + 1], qb[1][4 * m + 2], qb[1][4 * m + 3]};
                }
            }
        }
        BAR();
        int rem = 6 - 2 * c;
        if (c == 0) { BAR(); BAR(); rem -= 2; }
        STAMP(5);
        u32x4 pl[7][8];
        const __amdgpu_buffer_rsrc_t srs = __builtin_amdgcn_make_buffer_rsrc((void*)(ws_ + WS_S + (size_t)head * 8 * 32768), 0, 8 * 32768, 0x00020000);
        if (j > 0) {
            for (u32 spins = 0; spins < SPIN_CAP; ++spins) {
                u64 f = tag;
                if (lane < (u32)j) f = __hip_atomic_load(flags + lane, RLX_AGENT);
                if (__all(f == tag)) break;
                __builtin_amdgcn_s_sleep(1);
            }
            __builtin_amdgcn_fence(__ATOMIC_ACQUIRE, "wavefront");
            STAMP(6);
            if (j == 7 && c == 0 && lane == 0) __hip_atomic_store(flags + 16, tag, RLX_AGENT);
#pragma unroll
            for (int n = 0; n < 5; ++n)
                if (n < j) {
#pragma unroll
                    for (int f = 0; f < 8; ++f)
                        pl[n][f] = __builtin_amdgcn_raw_buffer_load_b128(srs, lane * 16 + (f & 3) * 1024, (j - 1 - n) * 32768 + c * 8192 + (f >> 2) * 4096, 16);
                }
        }
        for (int i = 0; i < rem; ++i) BAR();
        STAMP(7);
        if (j > 0) {
            f32x16 acc[4];
#pragma unroll
            for (int mt = 0; mt < 4; ++mt)
#pragma unroll
                for (int i = 0; i < 16; ++i) acc[mt][i] = 0.f;
            const u32 r = lane & 31, h = lane >> 5;
            const bool act = h == ((r >> 2) & 1);
            const u32 sel = act ? ((r >> 4) * 4 + ((r >> 3) & 1) * 2 + ((r >> 1) & 1)) : 99u;
            const u32 hm = (r & 1) ? 0xffff0000u : 0x0000ffffu;
#pragma unroll
            for (int n = 0; n < 7; ++n) {
                if (n < j) {
                    const f32x4 w4 = *(LAS f32x4*)(sm + L_W + n * 512 + r * 16);
#pragma unroll
                    for (int mt = 0; mt < 4; ++mt) {
                        const u32 wp = pk(w4[mt], w4[mt]) & hm;
#pragma unroll
                        for (int s = 0; s < 2; ++s) {
                            u32x4 dgl = {sel == 4 * s + 0 ? wp : 0u, sel == 4 * s + 1 ? wp : 0u, sel == 4 * s + 2 ? wp : 0u, sel == 4 * s + 3 ? wp : 0u};
                            acc[mt] = MFMA(__builtin_bit_cast(bf16x8, dgl), __builtin_bit_cast(bf16x8, pl[n][2 * mt + s]), acc[mt]);
                        }
                    }
                }
                if (n < 2 && n + 5 < j) {
#pragma unroll
                    for (int f = 0; f < 8; ++f)
                        pl[n + 5][f] = __builtin_amdgcn_raw_buffer_load_b128(srs, lane * 16 + (f & 3) * 1024, (j - 6 - n) * 32768 + c * 8192 + (f >> 2) * 4096, 16);
                }
            }
#pragma unroll
            for (int mt = 0; mt < 4; ++mt)
#pragma unroll
                for (int s = 0; s < 2; ++s) *(LAS bf16x8*)(sm + L_KT + c * 8192 + (2 * mt + s) * 1024 + lane * 16) = packstep(acc[mt], s);
        }
        STAMP(8);
        BAR();
        STAMP_FLUSH();
    } else {
        const int w = wave - 4;
        const u32 r = lane & 31, h = lane >> 5, blk = (lane >> 4) & 1, qq = (lane & 15) >> 2, pp = lane & 3;
        u64 tag;
        {
            u64 nn = __hip_atomic_load(flags + 16, RLX_AGENT);
            u32 lo = __builtin_amdgcn_readfirstlane((u32)nn), hi = __builtin_amdgcn_readfirstlane((u32)(nn >> 32));
            tag = mix_tag(((u64)hi << 32) | lo);
            if (w == 0 && lane == 0) { *(LAS u64*)(sm + L_MISC + 32) = tag; *(LAS u32*)(sm + L_MISC + 16) = 0u; }
        }
        BAR();
        STAMP(1);
        if (j < 7) {
            LAS float* dp = (LAS float*)(sm + L_DEC) + 32 * w + r;
            float dd = (dp[0] * dp[128]) * (dp[256] * dp[384]);
            if (lane < 32) __hip_atomic_store((GAS u64*)(ws + WS_D + (size_t)(head * 8 + j) * 1024) + r * 4 + w, ((u64)(u32)tag << 32) | __float_as_uint(dd), RLX_AGENT);
        }
        {
            const u32 voff = (lane >> 4) * 256 + (((lane & 15) ^ ((lane >> 4) << 2)) << 4);
            const char* vb = (const char*)v_ + (segrow + w * 16) * 256;
            const u32 ldsbase = (u32)(uintptr_t)(sm + L_V + w * 4096);
#pragma unroll
            for (int c = 0; c < 4; ++c) {
#define NTS " nt"
                asm volatile("s_mov_b32 m0, %2\n\ts_nop 0\n\t"
                             "global_load_lds_dwordx4 %0, %1" NTS "\n\t"
                             "global_load_lds_dwordx4 %0, %1 offset:1024" NTS "\n\t"
                             "global_load_lds_dwordx4 %0, %1 offset:2048" NTS "\n\t"
                             "global_load_lds_dwordx4 %0, %1 offset:3072" NTS
                             :: "v"(voff), "s"(vb + c * 16384), "s"(ldsbase + c * 16384) : "memory");
            }
        }
        const u32 fr = ((r & 2) << 1) | ((r >> 2) & 3);
        LAS char* krow[4];
#pragma unroll
        for (int ks = 0; ks < 4; ++ks) krow[ks] = sm + L_KT + r * 128 + (((2 * ks + h) ^ fr) << 4);
        const u32 eq = 4 * blk + (((pp & 1) << 1) | (pp >> 1));
        const u32 vcol = ((4 * (w ^ qq) + (eq >> 1)) << 4) + 8 * (eq & 1);
        LAS char* vnat = sm + L_V + (8 * h + qq) * 256 + vcol;
        LAS char* vper = sm + L_V + (4 * h + qq) * 256 + vcol;
        u32 tro[2][2];
#pragma unroll
        for (int tl = 0; tl < 2; ++tl)
#pragma unroll
            for (int jj = 0; jj < 2; ++jj)
                tro[tl][jj] = (8 * jj + 4 * h + qq) * 128 + (((4 * (tl ^ (qq >> 1))) + 2 * (blk ^ jj) + ((pp >> 1) ^ h)) << 4) + 8 * (pp & 1);
        LAS char* decp = sm + L_DEC + h * 16;
        BAR();
        STAMP(2);
        f32x16 W[4];
#pragma unroll
        for (int mt = 0; mt < 4; ++mt)
#pragma unroll
            for (int i = 0; i < 16; ++i) W[mt][i] = 0.f;
        bf16x8 Sf[5][8];
#pragma unroll
        for (int c = 0; c < 4; ++c) {
            if (c == 0) asm volatile("s_waitcnt vmcnt(12)" ::: "memory");
            if (c == 1) asm volatile("s_waitcnt vmcnt(8)" ::: "memory");
            if (c == 2) asm volatile("s_waitcnt vmcnt(4)" ::: "memory");
            if (c == 3) asm volatile("s_waitcnt vmcnt(0)" ::: "memory");
            BAR();
            STAMP(3 + c);
            bf16x8 Vf[4];
#pragma unroll
            for (int ks = 0; ks < 4; ++ks) Vf[ks] = trfrag(vnat + c * 16384 + ks * 4096, vnat + c * 16384 + ks * 4096 + 1024);
#pragma unroll
            for (int mt = 0; mt < 4; ++mt) {
#pragma unroll
                for (int ks = 0; ks < 4; ++ks) {
                    bf16x8 a = *(LAS bf16x8*)(krow[ks] + c * 16384 + mt * 4096);
                    W[mt] = MFMA(a, Vf[ks], W[mt]);
                }
            }
#pragma unroll
            for (int mt = 0; mt < 4; ++mt) {
#pragma unroll
                for (int gq = 0; gq < 4; ++gq) {
                    f32x4 dv = *(LAS f32x4*)(decp + c * 512 + mt * 128 + gq * 32);
#pragma unroll
                    for (int i = 0; i < 4; ++i) W[mt][4 * gq + i] *= dv[i];
                }
                Sf[c + 1][2 * mt] = packstep(W[mt], 0);
                Sf[c + 1][2 * mt + 1] = packstep(W[mt], 1);
                PIN(Sf[c + 1][2 * mt]); PIN(Sf[c + 1][2 * mt + 1]);
                if (c < 3) PIN(W[mt]);
            }
        }
        STAMP(7);
        if (j < 7) {
            const __amdgpu_buffer_rsrc_t prs = __builtin_amdgcn_make_buffer_rsrc((void*)(ws_ + WS_S + (size_t)(head * 8 + j) * 32768 + w * 8192), 0, 8192, 0x00020000);
#pragma unroll
            for (int f = 0; f < 8; ++f)
                __builtin_amdgcn_raw_buffer_store_b128(__builtin_bit_cast(u32x4, Sf[4][f]), prs, lane * 16 + (f & 3) * 1024, (f >> 2) * 4096, 16);
            asm volatile("s_waitcnt vmcnt(0)" ::: "memory");
            if (lane == 0) {
                u32 old = __hip_atomic_fetch_add((LAS u32*)(sm + L_MISC + 16), 1u, __ATOMIC_RELAXED, __HIP_MEMORY_SCOPE_WORKGROUP);
                if (old == 3) __hip_atomic_store(flags + j, tag, RLX_AGENT);
            }
        }
        STAMP(8);
        BAR();
        STAMP(9);
        f32x16 o[4][2];
        const u32 sg = w == 2 ? 1 : 0, tu = w == 0 ? 0 : 1;
        const u32 ka0 = sg ? tro[1][0] : tro[0][0], ka1 = sg ? tro[1][1] : tro[0][1];
        const u32 qa0 = tu ? tro[1][0] : tro[0][0], qa1 = tu ? tro[1][1] : tro[0][1];
        const int thr = (w == 1 ? 64 : (int)r) - 4 * (int)h;
#pragma unroll
        for (int c = 0; c < 4; ++c) {
            const u32 qslot = c == 0 ? L_X0 : L_KT + (c - 1) * 16384;
            if (w < 3) {
                f32x16 X;
#pragma unroll
                for (int i = 0; i < 16; ++i) X[i] = 0.f;
#pragma unroll
                for (int kk = 0; kk < 8; ++kk) {
                    bf16x8 a = trfrag(sm + L_KT + c * 16384 + kk * 2048 + ka0, sm + L_KT + c * 16384 + kk * 2048 + ka1);
                    bf16x8 bq = trfrag(sm + qslot + kk * 2048 + qa0, sm + qslot + kk * 2048 + qa1);
                    X = MFMA(a, bq, X);
                }
#pragma unroll
                for (int i = 0; i < 16; ++i) X[i] = ((i & 3) + 8 * (i >> 2)) <= thr ? X[i] : 0.f;
                *(LAS bf16x8*)(sm + L_AT + w * 2048 + lane * 16) = packstep(X, 0);
                *(LAS bf16x8*)(sm + L_AT + w * 2048 + 1024 + lane * 16) = packstep(X, 1);
            }
#pragma unroll
            for (int tl = 0; tl < 2; ++tl)
#pragma unroll
                for (int i = 0; i < 16; ++i) o[c][tl][i] = 0.f;
            if (c > 0) {
#pragma unroll
                for (int kk = 0; kk < 8; ++kk)
#pragma unroll
                    for (int tl = 0; tl < 2; ++tl)
                        o[c][tl] = MFMA(Sf[c][kk], trfrag(sm + qslot + kk * 2048 + tro[tl][0], sm + qslot + kk * 2048 + tro[tl][1]), o[c][tl]);
            }
            bf16x8 Vp[4];
#pragma unroll
            for (int ks = 0; ks < 4; ++ks) Vp[ks] = trfrag(vper + c * 16384 + ks * 4096, vper + c * 16384 + ks * 4096 + 2048);
            PIN(o[c][0]); PIN(o[c][1]);
            BAR();
            STAMP(10 + 2 * c);
            LAS char* at = sm + L_AT + lane * 16;
            o[c][0] = MFMA(Vp[0], *(LAS bf16x8*)(at), o[c][0]);
            o[c][1] = MFMA(Vp[0], *(LAS bf16x8*)(at + 2048), o[c][1]);
            o[c][0] = MFMA(Vp[1], *(LAS bf16x8*)(at + 1024), o[c][0]);
            o[c][1] = MFMA(Vp[1], *(LAS bf16x8*)(at + 3072), o[c][1]);
            o[c][1] = MFMA(Vp[2], *(LAS bf16x8*)(at + 4096), o[c][1]);
            o[c][1] = MFMA(Vp[3], *(LAS bf16x8*)(at + 5120), o[c][1]);
            PIN(o[c][0]); PIN(o[c][1]);
            if (c < 3) { BAR(); STAMP(11 + 2 * c); }
        }
        bf16x8 Sin[8];
        if (j > 0) {
#pragma unroll
            for (int kk = 0; kk < 8; ++kk) Sin[kk] = *(LAS bf16x8*)(sm + L_KT + w * 8192 + kk * 1024 + lane * 16);
        }
        GAS char* op = (GAS char*)out_ + (segrow + r) * 256 + w * 64 + h * 16;
#pragma unroll
        for (int c = 0; c < 4; ++c) {
            const u32 qslot = c == 0 ? L_X0 : L_V + (c - 1) * 16384;
#pragma unroll
            for (int tl = 0; tl < 2; ++tl) {
                if (j > 0) {
#pragma unroll
                    for (int kk = 0; kk < 8; ++kk)
                        o[c][tl] = MFMA(Sin[kk], trfrag(sm + qslot + kk * 2048 + tro[tl][0], sm + qslot + kk * 2048 + tro[tl][1]), o[c][tl]);
                }
                *(GAS bf16x8*)(op + (c * 64 + tl * 32) * 256) = packstep(o[c][tl], 0);
                *(GAS bf16x8*)(op + (c * 64 + tl * 32) * 256 + 32) = packstep(o[c][tl], 1);
            }
        }
        STAMP(18);
        { asm volatile("s_waitcnt vmcnt(0)" ::: "memory"); STAMP(19);
          u64 dl = pt0 + 4000 + 4 * (pt1 - pt0);
          while (__builtin_amdgcn_s_memrealtime() < dl) __builtin_amdgcn_s_sleep(2); }
    }
}

extern "C" void kernel_launch(void* const* d_in, const int* in_sizes, int n_in, void* d_out, int out_size, void* d_ws, size_t ws_size,
                              hipStream_t stream) {
    gla_kernel<<<256, 512, 0, stream>>>((const u32*)d_in[0], (const u32*)d_in[1], (const u32*)d_in[2], (const u32*)d_in[3], (u32*)d_out, (char*)d_ws);
}
```

```cpp
#define PROBE 0
#define OUT_AUX 16
#include <hip/hip_runtime.h>
#include <stdint.h>

typedef unsigned int u32;
typedef unsigned long long u64;
typedef u32 u32x2 __attribute__((ext_vector_type(2)));
typedef u32 u32x4 __attribute__((ext_vector_type(4)));
typedef float f32x2 __attribute__((ext_vector_type(2)));
typedef float f32x4 __attribute__((ext_vector_type(4)));
typedef float f32x16 __attribute__((ext_vector_type(16)));
typedef __bf16 bf16x2 __attribute__((ext_vector_type(2)));
typedef __bf16 bf16x8 __attribute__((ext_vector_type(8)));
typedef short s16x4 __attribute__((ext_vector_type(4)));
#define LAS __attribute__((address_space(3)))
#define GAS __attribute__((address_space(1)))
#define DI __device__ __forceinline__
#define RLX_AGENT __ATOMIC_RELAXED, __HIP_MEMORY_SCOPE_AGENT

#define NT_LOADS 1

constexpr int TT = 2048;
constexpr u32 L_KT = 0, L_V = 65536, L_X0 = 131072, L_AT = 147456, L_DEC = 153600, L_W = 155648, L_MISC = 159232, L_TOTAL = 159744;
constexpr size_t WS_D = 16384, WS_S = 1u << 20, WS_STAMP = 32u << 20;
constexpr float C0 = 0x1p-56f, PLIM = 0x1p56f;
constexpr float NLOG2E = -1.4426950408889634f, SCALE = 0.08838834764831845f;
constexpr u32 SPIN_CAP = 1u << 18;

#define BAR() asm volatile("s_waitcnt lgkmcnt(0)\n\ts_barrier" ::: "memory")
#define SCHED() __builtin_amdgcn_sched_barrier(0)
#define PIN(x) asm volatile("" : "+v"(x))
#define MFMA(a, b, c) __builtin_amdgcn_mfma_f32_32x32x16_bf16((a), (b), (c), 0, 0, 0)

#define STAMP(i) do { if ((i) == PROBE || (i) == 0) { u64 t_; asm volatile("s_memrealtime %0\n\ts_waitcnt lgkmcnt(0)" : "=s"(t_) :: "memory"); if ((i) == 0) pt0 = t_; if ((i) == PROBE) pt1 = t_; } } while (0)
#define STAMP_FLUSH() do {} while (0)

DI f32x2 unpk(u32 x) { return (f32x2){__uint_as_float(x << 16), __uint_as_float(x & 0xffff0000u)}; }
DI u32 pk(float a, float b) { return __builtin_bit_cast(u32, __builtin_convertvector((f32x2){a, b}, bf16x2)); }
DI u32 ldrow(__amdgpu_buffer_rsrc_t rs, u32 voff, int t) {
    return __builtin_amdgcn_raw_buffer_load_b32(rs, voff + (t & 15) * 256, (t >> 4) * 4096, NT_LOADS ? 2 : 0);
}
DI bf16x8 trfrag(LAS char* a0, LAS char* a1) {
    s16x4 lo = __builtin_amdgcn_ds_read_tr16_b64_v4i16((LAS s16x4*)a0);
    s16x4 hi = __builtin_amdgcn_ds_read_tr16_b64_v4i16((LAS s16x4*)a1);
    return __builtin_bit_cast(bf16x8, __builtin_shufflevector(lo, hi, 0, 1, 2, 3, 4, 5, 6, 7));
}
DI bf16x8 packstep(const f32x16& x, int s) {
    u32x4 p = {pk(x[8 * s], x[8 * s + 1]), pk(x[8 * s + 2], x[8 * s + 3]), pk(x[8 * s + 4], x[8 * s + 5]), pk(x[8 * s + 6], x[8 * s + 7])};
    return __builtin_bit_cast(bf16x8, p);
}
DI u64 mix_tag(u64 n) { return n * 6364136223846793005ull + 1442695040888963407ull; }

__global__ __launch_bounds__(512, 2) void gla_kernel(const u32* __restrict__ q_, const u32* __restrict__ k_, const u32* __restrict__ v_,
                                                     const u32* __restrict__ g_, u32* __restrict__ out_, char* __restrict__ ws_) {
    __shared__ __attribute__((aligned(16))) char smem_[L_TOTAL];
    LAS char* sm = (LAS char*)smem_;
    const u32 lane = threadIdx.x & 63;
    const int wave = __builtin_amdgcn_readfirstlane(threadIdx.x >> 6);
    const int b = blockIdx.x;
    const int head = (b & 7) * 4 + ((b >> 3) & 3), j = b >> 5;
    const size_t segrow = (size_t)head * TT + j * 256;
    GAS char* ws = (GAS char*)ws_;
    GAS u64* flags = (GAS u64*)(ws + head * 256);
    u64 pt0 = 0, pt1 = 0;
    STAMP(0);

    if (wave < 4) {
        const int c = wave;
        const size_t off = (segrow + c * 64) * 64;
        const __amdgpu_buffer_rsrc_t gp = __builtin_amdgcn_make_buffer_rsrc((void*)(g_ + off), 0, 16384, 0x00020000);
        const __amdgpu_buffer_rsrc_t kp = __builtin_amdgcn_make_buffer_rsrc((void*)(k_ + off), 0, 16384, 0x00020000);
        const __amdgpu_buffer_rsrc_t qp = __builtin_amdgcn_make_buffer_rsrc((void*)(q_ + off), 0, 16384, 0x00020000);
        const u32 l4 = lane * 4;
        u32 raw[64];
#pragma unroll
        for (int t = 0; t < 64; ++t) { raw[t] = ldrow(gp, l4, t); SCHED(); }
        f32x2 P[64];
        f32x2 p = {C0, C0};
#pragma unroll
        for (int t = 0; t < 64; ++t) {
            f32x2 e = unpk(raw[t]) * NLOG2E;
            e.x = __builtin_amdgcn_exp2f(e.x);
            e.y = __builtin_amdgcn_exp2f(e.y);
            p = __builtin_elementwise_fma(p, e, p);
            PIN(p);
            P[t] = p;
            raw[t] = ldrow(kp, l4, t);
            if ((t & 3) == 3) SCHED();
        }
        {
            f32x2 dec = {C0 * __builtin_amdgcn_rcpf(p.x), C0 * __builtin_amdgcn_rcpf(p.y)};
            *(LAS f32x2*)(sm + L_DEC + c * 512 + lane * 8) = dec;
            bool bad = !(p.x <= PLIM && p.y <= PLIM);
            u32 anybad = __ballot(bad) != 0;
            if (lane == 0) *(LAS u32*)(sm + L_MISC + c * 4) = anybad;
        }
        STAMP(1);
        BAR();
        const u32 fsw = ((lane & 1) << 2) | ((lane >> 1) & 3);
        LAS char* wrow = sm + lane * 256;
#pragma unroll
        for (int m = 0; m < 8; ++m) {
            u32x4 a, bb;
#pragma unroll
            for (int i = 0; i < 4; ++i) {
                const int t = 8 * m + 2 * i;
                f32x2 x0 = unpk(raw[t]) * P[t], x1 = unpk(raw[t + 1]) * P[t + 1];
                a[i] = pk(x0.x, x1.x);
                bb[i] = pk(x0.y, x1.y);
            }
            LAS char* dst = wrow + L_KT + c * 16384 + ((m ^ fsw) << 4);
            *(LAS u32x4*)dst = a;
            *(LAS u32x4*)(dst + 128) = bb;
            SCHED();
        }
        STAMP(2);
        BAR();
        const u64 tag = *(LAS u64*)(sm + L_MISC + 32);
        const u32 tag32 = (u32)tag;
        u32x4 dg[7];
        const __amdgpu_buffer_rsrc_t drs = __builtin_amdgcn_make_buffer_rsrc((void*)(ws_ + WS_D + (size_t)head * 8192), 0, 8192, 0x00020000);
        const u32 dvo = (lane & 31) * 32 + (lane >> 5) * 16;
        if (c == 3) {
#pragma unroll
            for (int n = 1; n < 7; ++n)
                if (n < j) dg[n] = __builtin_amdgcn_raw_buffer_load_b128(drs, dvo, (j - n) * 1024, 16);
        }
#pragma unroll
        for (int t = 0; t < 64; ++t) {
            P[t].x = __builtin_amdgcn_rcpf(P[t].x);
            P[t].y = __builtin_amdgcn_rcpf(P[t].y);
            P[t] *= SCALE;
            PIN(P[t]);
        }
        f32x2 F = {1.f, 1.f};
#pragma unroll
        for (int cc = 0; cc < 3; ++cc)
            if (cc < c) F *= *(LAS f32x2*)(sm + L_DEC + cc * 512 + lane * 8);
        for (int i = 0; i <= c; ++i) BAR();
#pragma unroll
        for (int t = 0; t < 64; ++t) { raw[t] = ldrow(qp, l4, t); SCHED(); }
        STAMP(3);
        if (c == 3) {
            f32x2 wv = {1.f, 1.f};
            *(LAS f32x2*)(sm + L_W + (lane & 31) * 16 + (lane >> 5) * 8) = wv;
#pragma unroll
            for (int n = 1; n < 7; ++n)
                if (n < j) {
                    u32x4 x = dg[n];
                    for (u32 spins = 0; !__all(x[1] == tag32 && x[3] == tag32) && spins < SPIN_CAP; ++spins) {
                        __builtin_amdgcn_s_sleep(2);
                        x = __builtin_amdgcn_raw_buffer_load_b128(drs, dvo, (j - n) * 1024, 16);
                    }
                    wv *= (f32x2){__uint_as_float(x[0]), __uint_as_float(x[2])};
                    *(LAS f32x2*)(sm + L_W + n * 512 + (lane & 31) * 16 + (lane >> 5) * 8) = wv;
                }
        }
        const int npre = (3 - c) + (c == 0 ? 0 : 2 * c - 1);
        for (int i = 0; i < npre; ++i) BAR();
        u32 qa[2][32], qb[2][32];
#pragma unroll
        for (int i = 0; i < 32; ++i) {
            const int t = 2 * i;
            f32x2 x0 = unpk(raw[t]) * P[t], x1 = unpk(raw[t + 1]) * P[t + 1];
            qa[0][i] = pk(x0.x, x1.x);
            qa[1][i] = pk(x0.y, x1.y);
            x0 *= F;
            x1 *= F;
            qb[0][i] = pk(x0.x, x1.x);
            qb[1][i] = pk(x0.y, x1.y);
            PIN(qa[0][i]); PIN(qa[1][i]); PIN(qb[0][i]); PIN(qb[1][i]);
            if ((i & 3) == 3) SCHED();
        }
        STAMP(4);
        if (c > 0) BAR();
        {
            const u32 slotA = c == 0 ? L_X0 : L_KT + (c - 1) * 16384;
#pragma unroll
            for (int m = 0; m < 8; ++m) {
                LAS char* dst = wrow + slotA + ((m ^ fsw) << 4);
                *(LAS u32x4*)dst = (u32x4){qa[0][4 * m], qa[0][4 * m + 1], qa[0][4 * m + 2], qa[0][4 * m + 3]};
                *(LAS u32x4*)(dst + 128) = (u32x4){qa[1][4 * m], qa[1][4 * m + 1], qa[1][4 * m + 2], qa[1][4 * m + 3]};
            }
            if (c > 0) {
                const u32 slotB = L_V + (c - 1) * 16384;
#pragma unroll
                for (int m = 0; m < 8; ++m) {
                    LAS char* dst = wrow + slotB + ((m ^ fsw) << 4);
                    *(LAS u32x4*)dst = (u32x4){qb[0][4 * m], qb[0][4 * m + 1], qb[0][4 * m + 2], qb[0][4 * m + 3]};
                    *(LAS u32x4*)(dst + 128) = (u32x4){qb[1][4 * m], qb[1][4 * m + 1], qb[1][4 * m + 2], qb[1][4 * m + 3]};
                }
            }
        }
        BAR();
        int rem = 6 - 2 * c;
        if (c == 0) { BAR(); BAR(); rem -= 2; }
        STAMP(5);
        u32x4 pl[7][8];
        const __amdgpu_buffer_rsrc_t srs = __builtin_amdgcn_make_buffer_rsrc((void*)(ws_ + WS_S + (size_t)head * 8 * 32768), 0, 8 * 32768, 0x00020000);
        if (j > 0) {
            for (u32 spins = 0; spins < SPIN_CAP; ++spins) {
                u64 f = tag;
                if (lane < (u32)j) f = __hip_atomic_load(flags + lane, RLX_AGENT);
                if (__all(f == tag)) break;
                __builtin_amdgcn_s_sleep(1);
            }
            __builtin_amdgcn_fence(__ATOMIC_ACQUIRE, "wavefront");
            STAMP(6);
            if (j == 7 && c == 0 && lane == 0) __hip_atomic_store(flags + 16, tag, RLX_AGENT);
#pragma unroll
            for (int n = 0; n < 5; ++n)
                if (n < j) {
#pragma unroll
                    for (int f = 0; f < 8; ++f)
                        pl[n][f] = __builtin_amdgcn_raw_buffer_load_b128(srs, lane * 16 + (f & 3) * 1024, (j - 1 - n) * 32768 + c * 8192 + (f >> 2) * 4096, 16);
                }
        }
        for (int i = 0; i < rem; ++i) BAR();
        STAMP(7);
        if (j > 0) {
            f32x16 acc[4];
#pragma unroll
            for (int mt = 0; mt < 4; ++mt)
#pragma unroll
                for (int i = 0; i < 16; ++i) acc[mt][i] = 0.f;
            const u32 r = lane & 31, h = lane >> 5;
            const bool act = h == ((r >> 2) & 1);
            const u32 sel = act ? ((r >> 4) * 4 + ((r >> 3) & 1) * 2 + ((r >> 1) & 1)) : 99u;
            const u32 hm = (r & 1) ? 0xffff0000u : 0x0000ffffu;
#pragma unroll
            for (int n = 0; n < 7; ++n) {
                if (n < j) {
                    const f32x4 w4 = *(LAS f32x4*)(sm + L_W + n * 512 + r * 16);
#pragma unroll
                    for (int mt = 0; mt < 4; ++mt) {
                        const u32 wp = pk(w4[mt], w4[mt]) & hm;
#pragma unroll
                        for (int s = 0; s < 2; ++s) {
                            u32x4 dgl = {sel == 4 * s + 0 ? wp : 0u, sel == 4 * s + 1 ? wp : 0u, sel == 4 * s + 2 ? wp : 0u, sel == 4 * s + 3 ? wp : 0u};
                            acc[mt] = MFMA(__builtin_bit_cast(bf16x8, dgl), __builtin_bit_cast(bf16x8, pl[n][2 * mt + s]), acc[mt]);
                        }
                    }
                }
                if (n < 2 && n + 5 < j) {
#pragma unroll
                    for (int f = 0; f < 8; ++f)
                        pl[n + 5][f] = __builtin_amdgcn_raw_buffer_load_b128(srs, lane * 16 + (f & 3) * 1024, (j - 6 - n) * 32768 + c * 8192 + (f >> 2) * 4096, 16);
                }
            }
#pragma unroll
            for (int mt = 0; mt < 4; ++mt)
#pragma unroll
                for (int s = 0; s < 2; ++s) *(LAS bf16x8*)(sm + L_KT + c * 8192 + (2 * mt + s) * 1024 + lane * 16) = packstep(acc[mt], s);
        }
        STAMP(8);
        BAR();
        STAMP_FLUSH();
    } else {
        const int w = wave - 4;
        const u32 r = lane & 31, h = lane >> 5, blk = (lane >> 4) & 1, qq = (lane & 15) >> 2, pp = lane & 3;
        u64 tag;
        {
            u64 nn = __hip_atomic_load(flags + 16, RLX_AGENT);
            u32 lo = __builtin_amdgcn_readfirstlane((u32)nn), hi = __builtin_amdgcn_readfirstlane((u32)(nn >> 32));
            tag = mix_tag(((u64)hi << 32) | lo);
            if (w == 0 && lane == 0) { *(LAS u64*)(sm + L_MISC + 32) = tag; *(LAS u32*)(sm + L_MISC + 16) = 0u; }
        }
        BAR();
        STAMP(1);
        if (j < 7) {
            LAS float* dp = (LAS float*)(sm + L_DEC) + 32 * w + r;
            float dd = (dp[0] * dp[128]) * (dp[256] * dp[384]);
            if (lane < 32) __hip_atomic_store((GAS u64*)(ws + WS_D + (size_t)(head * 8 + j) * 1024) + r * 4 + w, ((u64)(u32)tag << 32) | __float_as_uint(dd), RLX_AGENT);
        }
        {
            const u32 voff = (lane >> 4) * 256 + (((lane & 15) ^ ((lane >> 4) << 2)) << 4);
            const char* vb = (const char*)v_ + (segrow + w * 16) * 256;
            const u32 ldsbase = (u32)(uintptr_t)(sm + L_V + w * 4096);
#pragma unroll
            for (int c = 0; c < 4; ++c) {
#define NTS " nt"
                asm volatile("s_mov_b32 m0, %2\n\ts_nop 0\n\t"
                             "global_load_lds_dwordx4 %0, %1" NTS "\n\t"
                             "global_load_lds_dwordx4 %0, %1 offset:1024" NTS "\n\t"
                             "global_load_lds_dwordx4 %0, %1 offset:2048" NTS "\n\t"
                             "global_load_lds_dwordx4 %0, %1 offset:3072" NTS
                             :: "v"(voff), "s"(vb + c * 16384), "s"(ldsbase + c * 16384) : "memory");
            }
        }
        const u32 fr = ((r & 2) << 1) | ((r >> 2) & 3);
        LAS char* krow[4];
#pragma unroll
        for (int ks = 0; ks < 4; ++ks) krow[ks] = sm + L_KT + r * 128 + (((2 * ks + h) ^ fr) << 4);
        const u32 eq = 4 * blk + (((pp & 1) << 1) | (pp >> 1));
        const u32 vcol = ((4 * (w ^ qq) + (eq >> 1)) << 4) + 8 * (eq & 1);
        LAS char* vnat = sm + L_V + (8 * h + qq) * 256 + vcol;
        LAS char* vper = sm + L_V + (4 * h + qq) * 256 + vcol;
        u32 tro[2][2];
#pragma unroll
        for (int tl = 0; tl < 2; ++tl)
#pragma unroll
            for (int jj = 0; jj < 2; ++jj)
                tro[tl][jj] = (8 * jj + 4 * h + qq) * 128 + (((4 * (tl ^ (qq >> 1))) + 2 * (blk ^ jj) + ((pp >> 1) ^ h)) << 4) + 8 * (pp & 1);
        LAS char* decp = sm + L_DEC + h * 16;
        BAR();
        STAMP(2);
        f32x16 W[4];
#pragma unroll
        for (int mt = 0; mt < 4; ++mt)
#pragma unroll
            for (int i = 0; i < 16; ++i) W[mt][i] = 0.f;
        bf16x8 Sf[5][8];
#pragma unroll
        for (int c = 0; c < 4; ++c) {
            if (c == 0) asm volatile("s_waitcnt vmcnt(12)" ::: "memory");
            if (c == 1) asm volatile("s_waitcnt vmcnt(8)" ::: "memory");
            if (c == 2) asm volatile("s_waitcnt vmcnt(4)" ::: "memory");
            if (c == 3) asm volatile("s_waitcnt vmcnt(0)" ::: "memory");
            BAR();
            STAMP(3 + c);
            bf16x8 Vf[4];
#pragma unroll
            for (int ks = 0; ks < 4; ++ks) Vf[ks] = trfrag(vnat + c * 16384 + ks * 4096, vnat + c * 16384 + ks * 4096 + 1024);
#pragma unroll
            for (int mt = 0; mt < 4; ++mt) {
#pragma unroll
                for (int ks = 0; ks < 4; ++ks) {
                    bf16x8 a = *(LAS bf16x8*)(krow[ks] + c * 16384 + mt * 4096);
                    W[mt] = MFMA(a, Vf[ks], W[mt]);
                }
            }
#pragma unroll
            for (int mt = 0; mt < 4; ++mt) {
#pragma unroll
                for (int gq = 0; gq < 4; ++gq) {
                    f32x4 dv = *(LAS f32x4*)(decp + c * 512 + mt * 128 + gq * 32);
#pragma unroll
                    for (int i = 0; i < 4; ++i) W[mt][4 * gq + i] *= dv[i];
                }
                Sf[c + 1][2 * mt] = packstep(W[mt], 0);
                Sf[c + 1][2 * mt + 1] = packstep(W[mt], 1);
                PIN(Sf[c + 1][2 * mt]); PIN(Sf[c + 1][2 * mt + 1]);
                if (c < 3) PIN(W[mt]);
            }
        }
        STAMP(7);
        if (j < 7) {
            const __amdgpu_buffer_rsrc_t prs = __builtin_amdgcn_make_buffer_rsrc((void*)(ws_ + WS_S + (size_t)(head * 8 + j) * 32768 + w * 8192), 0, 8192, 0x00020000);
#pragma unroll
            for (int f = 0; f < 8; ++f)
                __builtin_amdgcn_raw_buffer_store_b128(__builtin_bit_cast(u32x4, Sf[4][f]), prs, lane * 16 + (f & 3) * 1024, (f >> 2) * 4096, 16);
            asm volatile("s_waitcnt vmcnt(0)" ::: "memory");
            if (lane == 0) {
                u32 old = __hip_atomic_fetch_add((LAS u32*)(sm + L_MISC + 16), 1u, __ATOMIC_RELAXED, __HIP_MEMORY_SCOPE_WORKGROUP);
                if (old == 3) __hip_atomic_store(flags + j, tag, RLX_AGENT);
            }
        }
        STAMP(8);
        BAR();
        STAMP(9);
        f32x16 o[4][2];
        const u32 sg = w == 2 ? 1 : 0, tu = w == 0 ? 0 : 1;
        const u32 ka0 = sg ? tro[1][0] : tro[0][0], ka1 = sg ? tro[1][1] : tro[0][1];
        const u32 qa0 = tu ? tro[1][0] : tro[0][0], qa1 = tu ? tro[1][1] : tro[0][1];
        const int thr = (w == 1 ? 64 : (int)r) - 4 * (int)h;
#pragma unroll
        for (int c = 0; c < 4; ++c) {
            const u32 qslot = c == 0 ? L_X0 : L_KT + (c - 1) * 16384;
            if (w < 3) {
                f32x16 X;
#pragma unroll
                for (int i = 0; i < 16; ++i) X[i] = 0.f;
#pragma unroll
                for (int kk = 0; kk < 8; ++kk) {
                    bf16x8 a = trfrag(sm + L_KT + c * 16384 + kk * 2048 + ka0, sm + L_KT + c * 16384 + kk * 2048 + ka1);
                    bf16x8 bq = trfrag(sm + qslot + kk * 2048 + qa0, sm + qslot + kk * 2048 + qa1);
                    X = MFMA(a, bq, X);
                }
#pragma unroll
                for (int i = 0; i < 16; ++i) X[i] = ((i & 3) + 8 * (i >> 2)) <= thr ? X[i] : 0.f;
                *(LAS bf16x8*)(sm + L_AT + w * 2048 + lane * 16) = packstep(X, 0);
                *(LAS bf16x8*)(sm + L_AT + w * 2048 + 1024 + lane * 16) = packstep(X, 1);
            }
#pragma unroll
            for (int tl = 0; tl < 2; ++tl)
#pragma unroll
                for (int i = 0; i < 16; ++i) o[c][tl][i] = 0.f;
            if (c > 0) {
#pragma unroll
                for (int kk = 0; kk < 8; ++kk)
#pragma unroll
                    for (int tl = 0; tl < 2; ++tl)
                        o[c][tl] = MFMA(Sf[c][kk], trfrag(sm + qslot + kk * 2048 + tro[tl][0], sm + qslot + kk * 2048 + tro[tl][1]), o[c][tl]);
            }
            bf16x8 Vp[4];
#pragma unroll
            for (int ks = 0; ks < 4; ++ks) Vp[ks] = trfrag(vper + c * 16384 + ks * 4096, vper + c * 16384 + ks * 4096 + 2048);
            PIN(o[c][0]); PIN(o[c][1]);
            BAR();
            STAMP(10 + 2 * c);
            LAS char* at = sm + L_AT + lane * 16;
            o[c][0] = MFMA(Vp[0], *(LAS bf16x8*)(at), o[c][0]);
            o[c][1] = MFMA(Vp[0], *(LAS bf16x8*)(at + 2048), o[c][1]);
            o[c][0] = MFMA(Vp[1], *(LAS bf16x8*)(at + 1024), o[c][0]);
            o[c][1] = MFMA(Vp[1], *(LAS bf16x8*)(at + 3072), o[c][1]);
            o[c][1] = MFMA(Vp[2], *(LAS bf16x8*)(at + 4096), o[c][1]);
            o[c][1] = MFMA(Vp[3], *(LAS bf16x8*)(at + 5120), o[c][1]);
            PIN(o[c][0]); PIN(o[c][1]);
            if (c < 3) { BAR(); STAMP(11 + 2 * c); }
        }
        bf16x8 Sin[8];
        if (j > 0) {
#pragma unroll
            for (int kk = 0; kk < 8; ++kk) Sin[kk] = *(LAS bf16x8*)(sm + L_KT + w * 8192 + kk * 1024 + lane * 16);
        }
        const __amdgpu_buffer_rsrc_t ors = __builtin_amdgcn_make_buffer_rsrc((void*)((char*)out_ + segrow * 256), 0, 65536, 0x00020000);
        GAS char* op = (GAS char*)out_ + (segrow + r) * 256 + w * 64 + h * 16;
#pragma unroll
        for (int c = 0; c < 4; ++c) {
            const u32 qslot = c == 0 ? L_X0 : L_V + (c - 1) * 16384;
#pragma unroll
            for (int tl = 0; tl < 2; ++tl) {
                if (j > 0) {
#pragma unroll
                    for (int kk = 0; kk < 8; ++kk)
                        o[c][tl] = MFMA(Sin[kk], trfrag(sm + qslot + kk * 2048 + tro[tl][0], sm + qslot + kk * 2048 + tro[tl][1]), o[c][tl]);
                }
                __builtin_amdgcn_raw_buffer_store_b128(__builtin_bit_cast(u32x4, packstep(o[c][tl], 0)), ors, r * 256 + w * 64 + h * 16, (c * 64 + tl * 32) * 256, OUT_AUX);
                __builtin_amdgcn_raw_buffer_store_b128(__builtin_bit_cast(u32x4, packstep(o[c][tl], 1)), ors, r * 256 + w * 64 + h * 16 + 32, (c * 64 + tl * 32) * 256, OUT_AUX);
            }
        }
        STAMP(18);
        { asm volatile("s_waitcnt vmcnt(0)" ::: "memory"); STAMP(19);
          u64 dl = pt0 + 4000 + 4 * (pt1 - pt0);
          while (__builtin_amdgcn_s_memrealtime() < dl) __builtin_amdgcn_s_sleep(2); }
    }
}

extern "C" void kernel_launch(void* const* d_in, const int* in_sizes, int n_in, void* d_out, int out_size, void* d_ws, size_t ws_size,
                              hipStream_t stream) {
    gla_kernel<<<256, 512, 0, stream>>>((const u32*)d_in[0], (const u32*)d_in[1], (const u32*)d_in[2], (const u32*)d_in[3], (u32*)d_out, (char*)d_ws);
}
```

```cpp
#define PROBE 0
#define OUT_AUX 16
#define TOPSTAMP
#include <hip/hip_runtime.h>
#include <stdint.h>

typedef unsigned int u32;
typedef unsigned long long u64;
typedef u32 u32x2 __attribute__((ext_vector_type(2)));
typedef u32 u32x4 __attribute__((ext_vector_type(4)));
typedef float f32x2 __attribute__((ext_vector_type(2)));
typedef float f32x4 __attribute__((ext_vector_type(4)));
typedef float f32x16 __attribute__((ext_vector_type(16)));
typedef __bf16 bf16x2 __attribute__((ext_vector_type(2)));
typedef __bf16 bf16x8 __attribute__((ext_vector_type(8)));
typedef short s16x4 __attribute__((ext_vector_type(4)));
#define LAS __attribute__((address_space(3)))
#define GAS __attribute__((address_space(1)))
#define DI __device__ __forceinline__
#define RLX_AGENT __ATOMIC_RELAXED, __HIP_MEMORY_SCOPE_AGENT

#define NT_LOADS 1

constexpr int TT = 2048;
constexpr u32 L_KT = 0, L_V = 65536, L_X0 = 131072, L_AT = 147456, L_DEC = 153600, L_W = 155648, L_MISC = 159232, L_TOTAL = 159744;
constexpr size_t WS_D = 16384, WS_S = 1u << 20, WS_STAMP = 32u << 20;
constexpr float C0 = 0x1p-56f, PLIM = 0x1p56f;
constexpr float NLOG2E = -1.4426950408889634f, SCALE = 0.08838834764831845f;
constexpr u32 SPIN_CAP = 1u << 18;

#define BAR() asm volatile("s_waitcnt lgkmcnt(0)\n\ts_barrier" ::: "memory")
#define SCHED() __builtin_amdgcn_sched_barrier(0)
#define PIN(x) asm volatile("" : "+v"(x))
#define MFMA(a, b, c) __builtin_amdgcn_mfma_f32_32x32x16_bf16((a), (b), (c), 0, 0, 0)

#define STAMP(i) do { if ((i) == PROBE || (i) == 0) { u64 t_; asm volatile("s_memrealtime %0\n\ts_waitcnt lgkmcnt(0)" : "=s"(t_) :: "memory"); if ((i) == 0) pt0 = t_; if ((i) == PROBE) pt1 = t_; } } while (0)
#define STAMP_FLUSH() do {} while (0)

DI f32x2 unpk(u32 x) { return (f32x2){__uint_as_float(x << 16), __uint_as_float(x & 0xffff0000u)}; }
DI u32 pk(float a, float b) { return __builtin_bit_cast(u32, __builtin_convertvector((f32x2){a, b}, bf16x2)); }
DI u32 ldrow(__amdgpu_buffer_rsrc_t rs, u32 voff, int t) {
    return __builtin_amdgcn_raw_buffer_load_b32(rs, voff + (t & 15) * 256, (t >> 4) * 4096, NT_LOADS ? 2 : 0);
}
DI bf16x8 trfrag(LAS char* a0, LAS char* a1) {
    s16x4 lo = __builtin_amdgcn_ds_read_tr16_b64_v4i16((LAS s16x4*)a0);
    s16x4 hi = __builtin_amdgcn_ds_read_tr16_b64_v4i16((LAS s16x4*)a1);
    return __builtin_bit_cast(bf16x8, __builtin_shufflevector(lo, hi, 0, 1, 2, 3, 4, 5, 6, 7));
}
DI bf16x8 packstep(const f32x16& x, int s) {
    u32x4 p = {pk(x[8 * s], x[8 * s + 1]), pk(x[8 * s + 2], x[8 * s + 3]), pk(x[8 * s + 4], x[8 * s + 5]), pk(x[8 * s + 6], x[8 * s + 7])};
    return __builtin_bit_cast(bf16x8, p);
}
DI u64 mix_tag(u64 n) { return n * 6364136223846793005ull + 1442695040888963407ull; }

__global__ __launch_bounds__(512, 2) void gla_kernel(const u32* __restrict__ q_, const u32* __restrict__ k_, const u32* __restrict__ v_,
                                                     const u32* __restrict__ g_, u32* __restrict__ out_, char* __restrict__ ws_) {
    __shared__ __attribute__((aligned(16))) char smem_[L_TOTAL];
    u64 pt0 = 0, pt1 = 0;
    STAMP(0);
    asm volatile("" : "+s"(ws_), "+s"(pt0));
    LAS char* sm = (LAS char*)smem_;
    const u32 lane = threadIdx.x & 63;
    const int wave = __builtin_amdgcn_readfirstlane(threadIdx.x >> 6);
    const int b = blockIdx.x;
    const int head = (b & 7) * 4 + ((b >> 3) & 3), j = b >> 5;
    const size_t segrow = (size_t)head * TT + j * 256;
    GAS char* ws = (GAS char*)ws_;
    GAS u64* flags = (GAS u64*)(ws + head * 256);
    asm volatile("" :: "s"(flags));
    STAMP(20);

    if (wave < 4) {
        const int c = wave;
        const size_t off = (segrow + c * 64) * 64;
        const __amdgpu_buffer_rsrc_t gp = __builtin_amdgcn_make_buffer_rsrc((void*)(g_ + off), 0, 16384, 0x00020000);
        const __amdgpu_buffer_rsrc_t kp = __builtin_amdgcn_make_buffer_rsrc((void*)(k_ + off), 0, 16384, 0x00020000);
        const __amdgpu_buffer_rsrc_t qp = __builtin_amdgcn_make_buffer_rsrc((void*)(q_ + off), 0, 16384, 0x00020000);
        const u32 l4 = lane * 4;
        u32 raw[64];
#pragma unroll
        for (int t = 0; t < 64; ++t) { raw[t] = ldrow(gp, l4, t); SCHED(); }
        f32x2 P[64];
        f32x2 p = {C0, C0};
#pragma unroll
        for (int t = 0; t < 64; ++t) {
            f32x2 e = unpk(raw[t]) * NLOG2E;
            e.x = __builtin_amdgcn_exp2f(e.x);
            e.y = __builtin_amdgcn_exp2f(e.y);
            p = __builtin_elementwise_fma(p, e, p);
            PIN(p);
            P[t] = p;
            raw[t] = ldrow(kp, l4, t);
            if ((t & 3) == 3) SCHED();
        }
        {
            f32x2 dec = {C0 * __builtin_amdgcn_rcpf(p.x), C0 * __builtin_amdgcn_rcpf(p.y)};
            *(LAS f32x2*)(sm + L_DEC + c * 512 + lane * 8) = dec;
            bool bad = !(p.x <= PLIM && p.y <= PLIM);
            u32 anybad = __ballot(bad) != 0;
            if (lane == 0) *(LAS u32*)(sm + L_MISC + c * 4) = anybad;
        }
        STAMP(1);
        BAR();
        const u32 fsw = ((lane & 1) << 2) | ((lane >> 1) & 3);
        LAS char* wrow = sm + lane * 256;
#pragma unroll
        for (int m = 0; m < 8; ++m) {
            u32x4 a, bb;
#pragma unroll
            for (int i = 0; i < 4; ++i) {
                const int t = 8 * m + 2 * i;
                f32x2 x0 = unpk(raw[t]) * P[t], x1 = unpk(raw[t + 1]) * P[t + 1];
                a[i] = pk(x0.x, x1.x);
                bb[i] = pk(x0.y, x1.y);
            }
            LAS char* dst = wrow + L_KT + c * 16384 + ((m ^ fsw) << 4);
            *(LAS u32x4*)dst = a;
            *(LAS u32x4*)(dst + 128) = bb;
            SCHED();
        }
        STAMP(2);
        BAR();
        const u64 tag = *(LAS u64*)(sm + L_MISC + 32);
        const u32 tag32 = (u32)tag;
        u32x4 dg[7];
        const __amdgpu_buffer_rsrc_t drs = __builtin_amdgcn_make_buffer_rsrc((void*)(ws_ + WS_D + (size_t)head * 8192), 0, 8192, 0x00020000);
        const u32 dvo = (lane & 31) * 32 + (lane >> 5) * 16;
        if (c == 3) {
#pragma unroll
            for (int n = 1; n < 7; ++n)
                if (n < j) dg[n] = __builtin_amdgcn_raw_buffer_load_b128(drs, dvo, (j - n) * 1024, 16);
        }
#pragma unroll
        for (int t = 0; t < 64; ++t) {
            P[t].x = __builtin_amdgcn_rcpf(P[t].x);
            P[t].y = __builtin_amdgcn_rcpf(P[t].y);
            P[t] *= SCALE;
            PIN(P[t]);
        }
        f32x2 F = {1.f, 1.f};
#pragma unroll
        for (int cc = 0; cc < 3; ++cc)
            if (cc < c) F *= *(LAS f32x2*)(sm + L_DEC + cc * 512 + lane * 8);
        for (int i = 0; i <= c; ++i) BAR();
#pragma unroll
        for (int t = 0; t < 64; ++t) { raw[t] = ldrow(qp, l4, t); SCHED(); }
        STAMP(3);
        if (c == 3) {
            f32x2 wv = {1.f, 1.f};
            *(LAS f32x2*)(sm + L_W + (lane & 31) * 16 + (lane >> 5) * 8) = wv;
#pragma unroll
            for (int n = 1; n < 7; ++n)
                if (n < j) {
                    u32x4 x = dg[n];
                    for (u32 spins = 0; !__all(x[1] == tag32 && x[3] == tag32) && spins < SPIN_CAP; ++spins) {
                        __builtin_amdgcn_s_sleep(2);
                        x = __builtin_amdgcn_raw_buffer_load_b128(drs, dvo, (j - n) * 1024, 16);
                    }
                    wv *= (f32x2){__uint_as_float(x[0]), __uint_as_float(x[2])};
                    *(LAS f32x2*)(sm + L_W + n * 512 + (lane & 31) * 16 + (lane >> 5) * 8) = wv;
                }
        }
        const int npre = (3 - c) + (c == 0 ? 0 : 2 * c - 1);
        for (int i = 0; i < npre; ++i) BAR();
        u32 qa[2][32], qb[2][32];
#pragma unroll
        for (int i = 0; i < 32; ++i) {
            const int t = 2 * i;
            f32x2 x0 = unpk(raw[t]) * P[t], x1 = unpk(raw[t + 1]) * P[t + 1];
            qa[0][i] = pk(x0.x, x1.x);
            qa[1][i] = pk(x0.y, x1.y);
            x0 *= F;
            x1 *= F;
            qb[0][i] = pk(x0.x, x1.x);
            qb[1][i] = pk(x0.y, x1.y);
            PIN(qa[0][i]); PIN(qa[1][i]); PIN(qb[0][i]); PIN(qb[1][i]);
            if ((i & 3) == 3) SCHED();
        }
        STAMP(4);
        if (c > 0) BAR();
        {
            const u32 slotA = c == 0 ? L_X0 : L_KT + (c - 1) * 16384;
#pragma unroll
            for (int m = 0; m < 8; ++m) {
                LAS char* dst = wrow + slotA + ((m ^ fsw) << 4);
                *(LAS u32x4*)dst = (u32x4){qa[0][4 * m], qa[0][4 * m + 1], qa[0][4 * m + 2], qa[0][4 * m + 3]};
                *(LAS u32x4*)(dst + 128) = (u32x4){qa[1][4 * m], qa[1][4 * m + 1], qa[1][4 * m + 2], qa[1][4 * m + 3]};
            }
            if (c > 0) {
                const u32 slotB = L_V + (c - 1) * 16384;
#pragma unroll
                for (int m = 0; m < 8; ++m) {
                    LAS char* dst = wrow + slotB + ((m ^ fsw) << 4);
                    *(LAS u32x4*)dst = (u32x4){qb[0][4 * m], qb[0][4 * m + 1], qb[0][4 * m + 2], qb[0][4 * m + 3]};
                    *(LAS u32x4*)(dst + 128) = (u32x4){qb[1][4 * m], qb[1][4 * m + 1], qb[1][4 * m + 2], qb[1][4 * m + 3]};
                }
            }
        }
        BAR();
        int rem = 6 - 2 * c;
        if (c == 0) { BAR(); BAR(); rem -= 2; }
        STAMP(5);
        u32x4 pl[7][8];
        const __amdgpu_buffer_rsrc_t srs = __builtin_amdgcn_make_buffer_rsrc((void*)(ws_ + WS_S + (size_t)head * 8 * 32768), 0, 8 * 32768, 0x00020000);
        if (j > 0) {
            for (u32 spins = 0; spins < SPIN_CAP; ++spins) {
                u64 f = tag;
                if (lane < (u32)j) f = __hip_atomic_load(flags + lane, RLX_AGENT);
                if (__all(f == tag)) break;
                __builtin_amdgcn_s_sleep(1);
            }
            __builtin_amdgcn_fence(__ATOMIC_ACQUIRE, "wavefront");
            STAMP(6);
            if (j == 7 && c == 0 && lane == 0) __hip_atomic_store(flags + 16, tag, RLX_AGENT);
#pragma unroll
            for (int n = 0; n < 5; ++n)
                if (n < j) {
#pragma unroll
                    for (int f = 0; f < 8; ++f)
                        pl[n][f] = __builtin_amdgcn_raw_buffer_load_b128(srs, lane * 16 + (f & 3) * 1024, (j - 1 - n) * 32768 + c * 8192 + (f >> 2) * 4096, 16);
                }
        }
        for (int i = 0; i < rem; ++i) BAR();
        STAMP(7);
        if (j > 0) {
            f32x16 acc[4];
#pragma unroll
            for (int mt = 0; mt < 4; ++mt)
#pragma unroll
                for (int i = 0; i < 16; ++i) acc[mt][i] = 0.f;
            const u32 r = lane & 31, h = lane >> 5;
            const bool act = h == ((r >> 2) & 1);
            const u32 sel = act ? ((r >> 4) * 4 + ((r >> 3) & 1) * 2 + ((r >> 1) & 1)) : 99u;
            const u32 hm = (r & 1) ? 0xffff0000u : 0x0000ffffu;
#pragma unroll
            for (int n = 0; n < 7; ++n) {
                if (n < j) {
                    const f32x4 w4 = *(LAS f32x4*)(sm + L_W + n * 512 + r * 16);
#pragma unroll
                    for (int mt = 0; mt < 4; ++mt) {
                        const u32 wp = pk(w4[mt], w4[mt]) & hm;
#pragma unroll
                        for (int s = 0; s < 2; ++s) {
                            u32x4 dgl = {sel == 4 * s + 0 ? wp : 0u, sel == 4 * s + 1 ? wp : 0u, sel == 4 * s + 2 ? wp : 0u, sel == 4 * s + 3 ? wp : 0u};
                            acc[mt] = MFMA(__builtin_bit_cast(bf16x8, dgl), __builtin_bit_cast(bf16x8, pl[n][2 * mt + s]), acc[mt]);
                        }
                    }
                }
                if (n < 2 && n + 5 < j) {
#pragma unroll
                    for (int f = 0; f < 8; ++f)
                        pl[n + 5][f] = __builtin_amdgcn_raw_buffer_load_b128(srs, lane * 16 + (f & 3) * 1024, (j - 6 - n) * 32768 + c * 8192 + (f >> 2) * 4096, 16);
                }
            }
#pragma unroll
            for (int mt = 0; mt < 4; ++mt)
#pragma unroll
                for (int s = 0; s < 2; ++s) *(LAS bf16x8*)(sm + L_KT + c * 8192 + (2 * mt + s) * 1024 + lane * 16) = packstep(acc[mt], s);
        }
        STAMP(8);
        BAR();
        STAMP_FLUSH();
    } else {
        const int w = wave - 4;
        const u32 r = lane & 31, h = lane >> 5, blk = (lane >> 4) & 1, qq = (lane & 15) >> 2, pp = lane & 3;
        u64 tag;
        {
            u64 nn = __hip_atomic_load(flags + 16, RLX_AGENT);
            u32 lo = __builtin_amdgcn_readfirstlane((u32)nn), hi = __builtin_amdgcn_readfirstlane((u32)(nn >> 32));
            tag = mix_tag(((u64)hi << 32) | lo);
            if (w == 0 && lane == 0) { *(LAS u64*)(sm + L_MISC + 32) = tag; *(LAS u32*)(sm + L_MISC + 16) = 0u; }
        }
        BAR();
        STAMP(1);
        if (j < 7) {
            LAS float* dp = (LAS float*)(sm + L_DEC) + 32 * w + r;
            float dd = (dp[0] * dp[128]) * (dp[256] * dp[384]);
            if (lane < 32) __hip_atomic_store((GAS u64*)(ws + WS_D + (size_t)(head * 8 + j) * 1024) + r * 4 + w, ((u64)(u32)tag << 32) | __float_as_uint(dd), RLX_AGENT);
        }
        {
            const u32 voff = (lane >> 4) * 256 + (((lane & 15) ^ ((lane >> 4) << 2)) << 4);
            const char* vb = (const char*)v_ + (segrow + w * 16) * 256;
            const u32 ldsbase = (u32)(uintptr_t)(sm + L_V + w * 4096);
#pragma unroll
            for (int c = 0; c < 4; ++c) {
#define NTS " nt"
                asm volatile("s_mov_b32 m0, %2\n\ts_nop 0\n\t"
                             "global_load_lds_dwordx4 %0, %1" NTS "\n\t"
                             "global_load_lds_dwordx4 %0, %1 offset:1024" NTS "\n\t"
                             "global_load_lds_dwordx4 %0, %1 offset:2048" NTS "\n\t"
                             "global_load_lds_dwordx4 %0, %1 offset:3072" NTS
                             :: "v"(voff), "s"(vb + c * 16384), "s"(ldsbase + c * 16384) : "memory");
            }
        }
        const u32 fr = ((r & 2) << 1) | ((r >> 2) & 3);
        LAS char* krow[4];
#pragma unroll
        for (int ks = 0; ks < 4; ++ks) krow[ks] = sm + L_KT + r * 128 + (((2 * ks + h) ^ fr) << 4);
        const u32 eq = 4 * blk + (((pp & 1) << 1) | (pp >> 1));
        const u32 vcol = ((4 * (w ^ qq) + (eq >> 1)) << 4) + 8 * (eq & 1);
        LAS char* vnat = sm + L_V + (8 * h + qq) * 256 + vcol;
        LAS char* vper = sm + L_V + (4 * h + qq) * 256 + vcol;
        u32 tro[2][2];
#pragma unroll
        for (int tl = 0; tl < 2; ++tl)
#pragma unroll
            for (int jj = 0; jj < 2; ++jj)
                tro[tl][jj] = (8 * jj + 4 * h + qq) * 128 + (((4 * (tl ^ (qq >> 1))) + 2 * (blk ^ jj) + ((pp >> 1) ^ h)) << 4) + 8 * (pp & 1);
        LAS char* decp = sm + L_DEC + h * 16;
        BAR();
        STAMP(2);
        f32x16 W[4];
#pragma unroll
        for (int mt = 0; mt < 4; ++mt)
#pragma unroll
            for (int i = 0; i < 16; ++i) W[mt][i] = 0.f;
        bf16x8 Sf[5][8];
#pragma unroll
        for (int c = 0; c < 4; ++c) {
            if (c == 0) asm volatile("s_waitcnt vmcnt(12)" ::: "memory");
            if (c == 1) asm volatile("s_waitcnt vmcnt(8)" ::: "memory");
            if (c == 2) asm volatile("s_waitcnt vmcnt(4)" ::: "memory");
            if (c == 3) asm volatile("s_waitcnt vmcnt(0)" ::: "memory");
            BAR();
            STAMP(3 + c);
            bf16x8 Vf[4];
#pragma unroll
            for (int ks = 0; ks < 4; ++ks) Vf[ks] = trfrag(vnat + c * 16384 + ks * 4096, vnat + c * 16384 + ks * 4096 + 1024);
#pragma unroll
            for (int mt = 0; mt < 4; ++mt) {
#pragma unroll
                for (int ks = 0; ks < 4; ++ks) {
                    bf16x8 a = *(LAS bf16x8*)(krow[ks] + c * 16384 + mt * 4096);
                    W[mt] = MFMA(a, Vf[ks], W[mt]);
                }
            }
#pragma unroll
            for (int mt = 0; mt < 4; ++mt) {
#pragma unroll
                for (int gq = 0; gq < 4; ++gq) {
                    f32x4 dv = *(LAS f32x4*)(decp + c * 512 + mt * 128 + gq * 32);
#pragma unroll
                    for (int i = 0; i < 4; ++i) W[mt][4 * gq + i] *= dv[i];
                }
                Sf[c + 1][2 * mt] = packstep(W[mt], 0);
                Sf[c + 1][2 * mt + 1] = packstep(W[mt], 1);
                PIN(Sf[c + 1][2 * mt]); PIN(Sf[c + 1][2 * mt + 1]);
                if (c < 3) PIN(W[mt]);
            }
        }
        STAMP(7);
        if (j < 7) {
            const __amdgpu_buffer_rsrc_t prs = __builtin_amdgcn_make_buffer_rsrc((void*)(ws_ + WS_S + (size_t)(head * 8 + j) * 32768 + w * 8192), 0, 8192, 0x00020000);
#pragma unroll
            for (int f = 0; f < 8; ++f)
                __builtin_amdgcn_raw_buffer_store_b128(__builtin_bit_cast(u32x4, Sf[4][f]), prs, lane * 16 + (f & 3) * 1024, (f >> 2) * 4096, 16);
            asm volatile("s_waitcnt vmcnt(0)" ::: "memory");
            if (lane == 0) {
                u32 old = __hip_atomic_fetch_add((LAS u32*)(sm + L_MISC + 16), 1u, __ATOMIC_RELAXED, __HIP_MEMORY_SCOPE_WORKGROUP);
                if (old == 3) __hip_atomic_store(flags + j, tag, RLX_AGENT);
            }
        }
        STAMP(8);
        BAR();
        STAMP(9);
        f32x16 o[4][2];
        const u32 sg = w == 2 ? 1 : 0, tu = w == 0 ? 0 : 1;
        const u32 ka0 = sg ? tro[1][0] : tro[0][0], ka1 = sg ? tro[1][1] : tro[0][1];
        const u32 qa0 = tu ? tro[1][0] : tro[0][0], qa1 = tu ? tro[1][1] : tro[0][1];
        const int thr = (w == 1 ? 64 : (int)r) - 4 * (int)h;
#pragma unroll
        for (int c = 0; c < 4; ++c) {
            const u32 qslot = c == 0 ? L_X0 : L_KT + (c - 1) * 16384;
            if (w < 3) {
                f32x16 X;
#pragma unroll
                for (int i = 0; i < 16; ++i) X[i] = 0.f;
#pragma unroll
                for (int kk = 0; kk < 8; ++kk) {
                    bf16x8 a = trfrag(sm + L_KT + c * 16384 + kk * 2048 + ka0, sm + L_KT + c * 16384 + kk * 2048 + ka1);
                    bf16x8 bq = trfrag(sm + qslot + kk * 2048 + qa0, sm + qslot + kk * 2048 + qa1);
                    X = MFMA(a, bq, X);
                }
#pragma unroll
                for (int i = 0; i < 16; ++i) X[i] = ((i & 3) + 8 * (i >> 2)) <= thr ? X[i] : 0.f;
                *(LAS bf16x8*)(sm + L_AT + w * 2048 + lane * 16) = packstep(X, 0);
                *(LAS bf16x8*)(sm + L_AT + w * 2048 + 1024 + lane * 16) = packstep(X, 1);
            }
#pragma unroll
            for (int tl = 0; tl < 2; ++tl)
#pragma unroll
                for (int i = 0; i < 16; ++i) o[c][tl][i] = 0.f;
            if (c > 0) {
#pragma unroll
                for (int kk = 0; kk < 8; ++kk)
#pragma unroll
                    for (int tl = 0; tl < 2; ++tl)
                        o[c][tl] = MFMA(Sf[c][kk], trfrag(sm + qslot + kk * 2048 + tro[tl][0], sm + qslot + kk * 2048 + tro[tl][1]), o[c][tl]);
            }
            bf16x8 Vp[4];
#pragma unroll
            for (int ks = 0; ks < 4; ++ks) Vp[ks] = trfrag(vper + c * 16384 + ks * 4096, vper + c * 16384 + ks * 4096 + 2048);
            PIN(o[c][0]); PIN(o[c][1]);
            BAR();
            STAMP(10 + 2 * c);
            LAS char* at = sm + L_AT + lane * 16;
            o[c][0] = MFMA(Vp[0], *(LAS bf16x8*)(at), o[c][0]);
            o[c][1] = MFMA(Vp[0], *(LAS bf16x8*)(at + 2048), o[c][1]);
            o[c][0] = MFMA(Vp[1], *(LAS bf16x8*)(at + 1024), o[c][0]);
            o[c][1] = MFMA(Vp[1], *(LAS bf16x8*)(at + 3072), o[c][1]);
            o[c][1] = MFMA(Vp[2], *(LAS bf16x8*)(at + 4096), o[c][1]);
            o[c][1] = MFMA(Vp[3], *(LAS bf16x8*)(at + 5120), o[c][1]);
            PIN(o[c][0]); PIN(o[c][1]);
            if (c < 3) { BAR(); STAMP(11 + 2 * c); }
        }
        bf16x8 Sin[8];
        if (j > 0) {
#pragma unroll
            for (int kk = 0; kk < 8; ++kk) Sin[kk] = *(LAS bf16x8*)(sm + L_KT + w * 8192 + kk * 1024 + lane * 16);
        }
        const __amdgpu_buffer_rsrc_t ors = __builtin_amdgcn_make_buffer_rsrc((void*)((char*)out_ + segrow * 256), 0, 65536, 0x00020000);
        GAS char* op = (GAS char*)out_ + (segrow + r) * 256 + w * 64 + h * 16;
#pragma unroll
        for (int c = 0; c < 4; ++c) {
            const u32 qslot = c == 0 ? L_X0 : L_V + (c - 1) * 16384;
#pragma unroll
            for (int tl = 0; tl < 2; ++tl) {
                if (j > 0) {
#pragma unroll
                    for (int kk = 0; kk < 8; ++kk)
                        o[c][tl] = MFMA(Sin[kk], trfrag(sm + qslot + kk * 2048 + tro[tl][0], sm + qslot + kk * 2048 + tro[tl][1]), o[c][tl]);
                }
                __builtin_amdgcn_raw_buffer_store_b128(__builtin_bit_cast(u32x4, packstep(o[c][tl], 0)), ors, r * 256 + w * 64 + h * 16, (c * 64 + tl * 32) * 256, OUT_AUX);
                __builtin_amdgcn_raw_buffer_store_b128(__builtin_bit_cast(u32x4, packstep(o[c][tl], 1)), ors, r * 256 + w * 64 + h * 16 + 32, (c * 64 + tl * 32) * 256, OUT_AUX);
            }
        }
        STAMP(18);
        { asm volatile("s_waitcnt vmcnt(0)" ::: "memory"); STAMP(19);

#define DL 4000
          u64 dl = pt0 + DL + 4 * (pt1 - pt0);
          while (__builtin_amdgcn_s_memrealtime() < dl) __builtin_amdgcn_s_sleep(2); }
    }
}

extern "C" void kernel_launch(void* const* d_in, const int* in_sizes, int n_in, void* d_out, int out_size, void* d_ws, size_t ws_size,
                              hipStream_t stream) {
    gla_kernel<<<256, 512, 0, stream>>>((const u32*)d_in[0], (const u32*)d_in[1], (const u32*)d_in[2], (const u32*)d_in[3], (u32*)d_out, (char*)d_ws);
}
```

```cpp
#define GRID 128
#include <hip/hip_runtime.h>
#include <hip/hip_bf16.h>
typedef unsigned short u16;
__device__ __forceinline__ float bf2f(u16 x) { return __uint_as_float((unsigned)x << 16); }
__device__ __forceinline__ u16 f2bf(float x) { __hip_bfloat16 b = __float2bfloat16(x); return *(u16*)&b; }

__global__ __launch_bounds__(128) void naive_kernel(const u16* q, const u16* k, const u16* v, const u16* g, u16* out, int T) {
    unsigned long long pt0; asm volatile("s_memrealtime %0\n\ts_waitcnt lgkmcnt(0)" : "=s"(pt0) :: "memory");
    if (blockIdx.x >= 128) { while (__builtin_amdgcn_s_memrealtime() < pt0 + 230000) __builtin_amdgcn_s_sleep(2); return; }
    const int h = blockIdx.x >> 2, es = blockIdx.x & 3;
    const int tid = threadIdx.x, dq = tid >> 5, e = (tid & 31) + 32 * es;
    __shared__ float sq[128], sk[128], sa[128], so[4][32];
    float S[32];
#pragma unroll
    for (int i = 0; i < 32; ++i) S[i] = 0.f;
    const float scale = 0.08838834764831845f;
    for (int t = 0; t < T; ++t) {
        size_t base = ((size_t)h * T + t) * 128;
        __syncthreads();
        sq[tid] = bf2f(q[base + tid]);
        sk[tid] = bf2f(k[base + tid]);
        float gg = bf2f(g[base + tid]);
        sa[tid] = 1.f / (1.f + __expf(-gg));
        float vv = bf2f(v[base + e]);
        __syncthreads();
        float o = 0.f;
#pragma unroll
        for (int i = 0; i < 32; ++i) {
            int d = dq * 32 + i;
            S[i] = sa[d] * S[i] + sk[d] * vv;
            o += sq[d] * S[i];
        }
        so[dq][tid & 31] = o;
        __syncthreads();
        if (tid < 32) __hip_atomic_store(out + base + e, f2bf((so[0][tid] + so[1][tid] + so[2][tid] + so[3][tid]) * scale), __ATOMIC_RELAXED, __HIP_MEMORY_SCOPE_AGENT);
    }
    asm volatile("s_waitcnt vmcnt(0)" ::: "memory");
    while (__builtin_amdgcn_s_memrealtime() < pt0 + 230000) __builtin_amdgcn_s_sleep(2);
}

extern "C" void kernel_launch(void* const* d_in, const int* in_sizes, int n_in,
                              void* d_out, int out_size, void* d_ws, size_t ws_size,
                              hipStream_t stream) {
    naive_kernel<<<GRID, 128, 0, stream>>>((const u16*)d_in[0], (const u16*)d_in[1], (const u16*)d_in[2], (const u16*)d_in[3], (u16*)d_out, 2048);
}
```

```cpp
#include <hip/hip_runtime.h>
#include <stdint.h>

typedef unsigned int u32;
typedef unsigned long long u64;
typedef u32 u32x2 __attribute__((ext_vector_type(2)));
typedef u32 u32x4 __attribute__((ext_vector_type(4)));
typedef float f32x2 __attribute__((ext_vector_type(2)));
typedef float f32x4 __attribute__((ext_vector_type(4)));
typedef float f32x16 __attribute__((ext_vector_type(16)));
typedef __bf16 bf16x2 __attribute__((ext_vector_type(2)));
typedef __bf16 bf16x8 __attribute__((ext_vector_type(8)));
typedef short s16x4 __attribute__((ext_vector_type(4)));
#define LAS __attribute__((address_space(3)))
#define GAS __attribute__((address_space(1)))
#define DI __device__ __forceinline__
#define RLX_AGENT __ATOMIC_RELAXED, __HIP_MEMORY_SCOPE_AGENT
#define RLX_WG __ATOMIC_RELAXED, __HIP_MEMORY_SCOPE_WORKGROUP

constexpr int TT = 2048;
constexpr u32 L_X0 = 0, L_KT = 16384, L_V = 81920, L_AT = 147456, L_DEC = 159744, L_WB = 161792, L_FLG = 163584, L_TOTAL = 163840, L_TAG = L_AT + 6144;
enum {
    F_GI = 0,
    F_GD = 1,
    F_VF = 4,
    F_QB = 5,
    F_WR = 6,
    F_PUB = 7,
    F_KR = 8,
    F_VL = 12,
    F_QR = 16,
    F_AT = 20,
    F_SR = 24,
    F_OC = 28,
    F_BAD = 32,
    F_KL = 40
};
constexpr size_t WS_D = 65536, WS_S = 1u << 20;
constexpr float C0 = 0x1p-56f, PLIM = 0x1p56f;
constexpr float NLOG2E = -1.4426950408889634f, SCALE = 0.08838834764831845f;
constexpr u32 SPIN_CAP = 1u << 18;
constexpr int WIN = 2;

#define OSLOT(c) ((c) == 0 ? L_KT + 49152 : (c) == 1 ? L_V + 16384 : (c) == 2 ? L_V : L_X0)
#define BAR() asm volatile("s_waitcnt lgkmcnt(0)\n\ts_barrier" ::: "memory")
#define SCHED() __builtin_amdgcn_sched_barrier(0)
#define MEMFENCE() asm volatile("" ::: "memory")
#define PIN(x) asm volatile("" : "+v"(x))
#define MFMA(a, b, c) __builtin_amdgcn_mfma_f32_32x32x16_bf16((a), (b), (c), 0, 0, 0)
#define UNROLL _Pragma("unroll")

DI f32x2 unpk(u32 x) { return (f32x2){__uint_as_float(x << 16), __uint_as_float(x & 0xffff0000u)}; }
DI u32 pk(float a, float b) { return __builtin_bit_cast(u32, __builtin_convertvector((f32x2){a, b}, bf16x2)); }
DI f32x2 fma2(f32x2 a, f32x2 b, f32x2 c) { return __builtin_elementwise_fma(a, b, c); }
DI u32x2 ldrow(__amdgpu_buffer_rsrc_t rs, u32 voff, int i) {
    return __builtin_amdgcn_raw_buffer_load_b64(rs, voff + (i & 15) * 256, (i >> 4) * 4096, 2);
}
DI bf16x8 trfrag(LAS char* a0, LAS char* a1) {
    s16x4 lo = __builtin_amdgcn_ds_read_tr16_b64_v4i16((LAS s16x4*)a0);
    s16x4 hi = __builtin_amdgcn_ds_read_tr16_b64_v4i16((LAS s16x4*)a1);
    return __builtin_bit_cast(bf16x8, __builtin_shufflevector(lo, hi, 0, 1, 2, 3, 4, 5, 6, 7));
}
DI bf16x8 packstep(const f32x16& x, int s) {
    u32x4 p = {pk(x[8 * s], x[8 * s + 1]), pk(x[8 * s + 2], x[8 * s + 3]), pk(x[8 * s + 4], x[8 * s + 5]), pk(x[8 * s + 6], x[8 * s + 7])};
    return __builtin_bit_cast(bf16x8, p);
}
template <int I> DI float rowbc(float d) {
    return __builtin_bit_cast(float, __builtin_amdgcn_update_dpp(0, __builtin_bit_cast(int, d), 0x150 + I, 0xf, 0xf, true));
}
template <int Q> DI void scale4(f32x16& x, float d) {
    float t0 = x[4 * Q + 0] * rowbc<4 * Q + 0>(d), t1 = x[4 * Q + 1] * rowbc<4 * Q + 1>(d), t2 = x[4 * Q + 2] * rowbc<4 * Q + 2>(d), t3 = x[4 * Q + 3] * rowbc<4 * Q + 3>(d);
    asm volatile("" : "+v"(t0), "+v"(t1), "+v"(t2), "+v"(t3));
    x[4 * Q + 0] = t0; x[4 * Q + 1] = t1; x[4 * Q + 2] = t2; x[4 * Q + 3] = t3;
}
__host__ __device__ constexpr int ord_kk(int s) { return s < 5 ? s : s < 10 ? s - 5 : 5 + ((s - 10) >> 1); }
__host__ __device__ constexpr int ord_tl(int s) { return s < 5 ? 0 : s < 10 ? 1 : (s & 1); }
template <int N> struct IntC { static constexpr int value = N; };
template <int I, int N, class F> DI void static_for(F&& f) { if constexpr (I < N) { f(IntC<I>{}); static_for<I + 1, N>(f); } }
DI u32 lane_id() { u32 l; asm volatile("v_mbcnt_lo_u32_b32 %0, -1, 0\n\tv_mbcnt_hi_u32_b32 %0, -1, %0" : "=v"(l)); __builtin_assume(l < 64); return l; }
DI u64 mix_tag(u64 n) { return n * 6364136223846793005ull + 1442695040888963407ull; }
DI void sig(u32 fb, int f) {
    u32 t0, t1;
    asm volatile("v_mov_b32 %0, %2\n\tv_mov_b32 %1, 1\n\ts_mov_b64 exec, 1\n\tds_add_u32 %0, %1\n\ts_mov_b64 exec, -1"
                 : "=&v"(t0), "=&v"(t1) : "s"(fb + 4 * f) : "memory");
}
DI void wait_ge(u32 fb, int f, u32 n) {
    u32 t0, t1, s0, cnt;
    asm volatile("v_mov_b32 %0, %4\n\ts_mov_b32 %3, 0x400000\n"
                 "1:\n\tds_read_b32 %1, %0\n\ts_waitcnt lgkmcnt(0)\n\tv_readfirstlane_b32 %2, %1\n\ts_nop 0\n\ts_cmp_ge_u32 %2, %5\n\ts_cbranch_scc1 2f\n\t"
                 "s_sleep 1\n\ts_sub_u32 %3, %3, 1\n\ts_cbranch_scc0 1b\n"
                 "2:"
                 : "=&v"(t0), "=&v"(t1), "=&s"(s0), "=&s"(cnt) : "s"(fb + 4 * f), "s"(n) : "memory", "scc");
}
#define FLAG(f) __hip_atomic_load((LAS u32*)(sm + L_FLG) + (f), RLX_WG)
#define NOT_YET(v, n) __builtin_expect((u32)__builtin_amdgcn_readfirstlane(v) < (u32)(n), 0)
#define SLOW_WAIT(f, n) do { __builtin_amdgcn_s_setprio(0); wait_ge(fb, f, n); __builtin_amdgcn_s_setprio(3); } while (0)

DI bool any_bad(LAS char* sm) {
    const u32x4 f = *(volatile LAS u32x4*)(sm + L_FLG + F_BAD * 4);
    return __builtin_expect(__builtin_amdgcn_readfirstlane(f[0] | f[1] | f[2] | f[3]) != 0, 0);
}

template <bool OUT>
DI void slow_pass(const u32* q_, const u32* k_, const u32* v_, const u32* g_, u32* out_, LAS char* sm, size_t segrow, float (&S)[32]) {
    const u32 t = threadIdx.x, e = t & 127, dq = __builtin_amdgcn_readfirstlane(t >> 7);
    LAS float* la = (LAS float*)sm;
    LAS float* lk = la + 2048;
    LAS float* lq = la + 4096;
    LAS float* lv = la + 6144;
    LAS float* po = la + 8192;
    _Pragma("nounroll") for (int blk = 0; blk < 16; ++blk) {
        __syncthreads();
        UNROLL for (int r = 0; r < 2; ++r) {
            const u32 idx = t + 512 * r;
            const size_t gi = (segrow + blk * 16) * 64 + idx;
            const f32x2 gg = unpk(g_[gi]), kk = unpk(k_[gi]), vv = unpk(v_[gi]);
            *(LAS f32x2*)(la + 2 * idx) = (f32x2){1.f / (1.f + __expf(-gg.x)), 1.f / (1.f + __expf(-gg.y))};
            *(LAS f32x2*)(lk + 2 * idx) = kk;
            *(LAS f32x2*)(lv + 2 * idx) = vv;
            if (OUT) *(LAS f32x2*)(lq + 2 * idx) = unpk(q_[gi]);
        }
        __syncthreads();
        _Pragma("nounroll") for (int tk = 0; tk < 16; ++tk) {
            const float vv = lv[tk * 128 + e];
            float o = 0.f;
            UNROLL for (int i4 = 0; i4 < 8; ++i4) {
                const f32x4 a4 = *(LAS f32x4*)(la + tk * 128 + dq * 32 + i4 * 4), k4 = *(LAS f32x4*)(lk + tk * 128 + dq * 32 + i4 * 4);
                f32x4 q4;
                if (OUT) q4 = *(LAS f32x4*)(lq + tk * 128 + dq * 32 + i4 * 4);
                UNROLL for (int x = 0; x < 4; ++x) {
                    S[4 * i4 + x] = a4[x] * S[4 * i4 + x] + k4[x] * vv;
                    if (OUT) o += q4[x] * S[4 * i4 + x];
                }
            }
            if (OUT) po[(tk * 4 + dq) * 128 + e] = o;
        }
        if (OUT) {
            __syncthreads();
            UNROLL for (int r = 0; r < 2; ++r) {
                const u32 idx = t + 512 * r, tk = idx >> 6, e2 = (idx & 63) * 2;
                f32x2 o = {0.f, 0.f};
                UNROLL for (int x = 0; x < 4; ++x) o += *(LAS f32x2*)(po + (tk * 4 + x) * 128 + e2);
                out_[(segrow + blk * 16) * 64 + idx] = pk(o.x * SCALE, o.y * SCALE);
            }
        }
    }
}
DI void slow_segment(const u32* q_, const u32* k_, const u32* v_, const u32* g_, u32* out_, char* ws_, LAS char* sm, int head, int j) {
    __builtin_amdgcn_s_setprio(0);
    asm volatile("s_waitcnt vmcnt(0)" ::: "memory");
    const u64 tag = *(volatile LAS u64*)(sm + L_TAG);
    const size_t segrow = (size_t)head * TT + j * 256;
    const u32 t = threadIdx.x, e = t & 127, dq = t >> 7;
    GAS u64* flags = (GAS u64*)((GAS char*)ws_ + head * 2048);
    const __amdgpu_buffer_rsrc_t srs = __builtin_amdgcn_make_buffer_rsrc((void*)(ws_ + WS_S + (size_t)head * 8 * 32768), 0, 8 * 32768, 0x00020000);
    const u32 eq = (e >> 2) & 7, cq = (eq & 4) | ((eq & 1) << 1) | ((eq >> 1) & 1);
    const u32 uoff = dq * 8192 + (e >> 5) * 1024 + (cq * 4 + (e & 3)) * 16;
    float S[32];
    UNROLL for (int i = 0; i < 32; ++i) S[i] = 0.f;
    if (j < 7) {
        slow_pass<false>(q_, k_, v_, g_, out_, sm, segrow, S);
        UNROLL for (int u = 0; u < 4; ++u) {
            const int i0 = 16 * (u >> 1) + 4 * (u & 1);
            const u32x4 x = {pk(C0 * S[i0], C0 * S[i0 + 1]), pk(C0 * S[i0 + 2], C0 * S[i0 + 3]), pk(C0 * S[i0 + 8], C0 * S[i0 + 9]), pk(C0 * S[i0 + 10], C0 * S[i0 + 11])};
            __builtin_amdgcn_raw_buffer_store_b128(x, srs, uoff + (u >> 1) * 4096 + (u & 1) * 512, j * 32768, 16);
        }
        asm volatile("s_waitcnt vmcnt(0)" ::: "memory");
        __syncthreads();
        if (t == 0) __hip_atomic_store(flags + j * 16, tag, RLX_AGENT);
        UNROLL for (int i = 0; i < 32; ++i) S[i] = 0.f;
    }
    if (j > 0) {
        __syncthreads();
        LAS float* ld = (LAS float*)sm;
        _Pragma("nounroll") for (u32 x = t; x < (u32)j * 128; x += 512) {
            const GAS u64* gq = (const GAS u64*)((GAS char*)ws_ + WS_D + (size_t)head * 8192) + x;
            u64 y = 0;
            _Pragma("nounroll") for (u32 spins = 0; spins < SPIN_CAP; ++spins) {
                y = __hip_atomic_load(gq, RLX_AGENT);
                if ((u32)(y >> 32) == (u32)tag) break;
                __builtin_amdgcn_s_sleep(2);
            }
            ld[(x & ~127u) + ((x & 7) << 4) + ((x >> 3) & 15)] = __uint_as_float((u32)y);
        }
        if (t < (u32)j) {
            _Pragma("nounroll") for (u32 spins = 0; spins < SPIN_CAP; ++spins) {
                if (__hip_atomic_load(flags + t * 16, RLX_AGENT) == tag) break;
                __builtin_amdgcn_s_sleep(8);
            }
        }
        __syncthreads();
        if (j == 7 && t == 0) __hip_atomic_store(flags + 128, tag, RLX_AGENT);
        _Pragma("nounroll") for (int i = 0; i < j; ++i) {
            UNROLL for (int u = 0; u < 4; ++u) {
                const u32x4 x = __builtin_amdgcn_raw_buffer_load_b128(srs, uoff + (u >> 1) * 4096 + (u & 1) * 512, i * 32768, 16);
                UNROLL for (int y = 0; y < 4; ++y) {
                    const int i0 = 16 * (u >> 1) + 4 * (u & 1) + 8 * (y >> 1) + 2 * (y & 1);
                    const f32x2 z = unpk(x[y]);
                    S[i0] = ld[i * 128 + dq * 32 + i0] * S[i0] + z.x;
                    S[i0 + 1] = ld[i * 128 + dq * 32 + i0 + 1] * S[i0 + 1] + z.y;
                }
            }
        }
        UNROLL for (int i = 0; i < 32; ++i) S[i] *= 1.f / C0;
    }
    slow_pass<true>(q_, k_, v_, g_, out_, sm, segrow, S);
}

__global__ __launch_bounds__(512, 2) void gla_kernel(const u32* __restrict__ q_, const u32* __restrict__ k_, const u32* __restrict__ v_,
                                                     const u32* __restrict__ g_, u32* __restrict__ out_, char* __restrict__ ws_) {
    __shared__ __attribute__((aligned(16))) char smem_[L_TOTAL];
    LAS char* sm = (LAS char*)smem_;
    const u32 fb = (u32)(uintptr_t)(sm + L_FLG);
    const u32 lane = threadIdx.x & 63;
    const int wave = __builtin_amdgcn_readfirstlane(threadIdx.x >> 6);
    const int b = blockIdx.x;
    {
            u64 pc; u32 len;
            asm volatile("s_getpc_b64 %0\n.Lgla_pc:\n\ts_mov_b32 %1, .Lgla_text_end-.Lgla_pc" : "=s"(pc), "=s"(len));
            len = (int)len > 16 ? len : 16u;
            const u32 coff = min((((u32)b & 56) + wave) * 1024 + lane * 16, len - 16);
            asm volatile("s_mov_b32 m0, %2\n\ts_nop 0\n\tglobal_load_lds_dwordx4 %0, %1" :: "v"(coff), "s"(pc), "s"((u32)(uintptr_t)(sm + L_AT + (wave & 3) * 1024)) : "memory");
            SCHED();
    }
    const int head = (b & 7) * 4 + ((b >> 3) & 3), j = b >> 5;
    const size_t segrow = (size_t)head * TT + j * 256;
    GAS char* ws = (GAS char*)ws_;
    GAS u64* flags = (GAS u64*)(ws + head * 2048);

    if (wave >= 4) {
        const int w = wave - 4;
        if (w == 0) *((LAS u32*)(sm + L_FLG) + lane) = 0u;
        {
            const u32 junk_lds = (u32)(uintptr_t)(sm + L_AT + w * 1024);
            const char* tp = w == 0 ? (const char*)q_ + segrow * 256 : w == 1 ? (const char*)out_ + segrow * 256 : ws_ + WS_S + (size_t)(head * 8 + 7) * 32768;
            const char* tp2 = (const char*)(w & 1 ? v_ : k_) + segrow * 256;
            asm volatile("s_mov_b32 m0, %3\n\ts_nop 0\n\tglobal_load_lds_dword %0, %1 nt\n\tglobal_load_lds_dword %0, %2"
                         :: "v"(lane * 4), "s"(tp), "s"(tp2), "s"(junk_lds) : "memory");
        }
        BAR();
    }
    int role = wave;
    asm volatile("" : "+s"(role));
    if (__builtin_expect(role < 4, 1)) {
        const int c = wave;
        const u32 hh = lane >> 5, l5 = lane & 31;
        const size_t off = (segrow + c * 64) * 64;
        const __amdgpu_buffer_rsrc_t gp = __builtin_amdgcn_make_buffer_rsrc((void*)(g_ + off), 0, 16384, 0x00020000);
        const __amdgpu_buffer_rsrc_t qp = __builtin_amdgcn_make_buffer_rsrc((void*)(q_ + off), 0, 16384, 0x00020000);
        u32 lo = hh * 8192 + l5 * 8;
        PIN(lo);
        if (c == 0) __builtin_amdgcn_s_setprio(3); else if (c == 1) __builtin_amdgcn_s_setprio(2); else if (c == 2) __builtin_amdgcn_s_setprio(1);
        u32x2 gr[32], raw[32];
        const u64 nn = __hip_atomic_load(flags + 128, RLX_AGENT);
        SCHED();
        UNROLL for (int i = 0; i < 8; ++i) { gr[i] = ldrow(gp, lo, i); SCHED(); }
        BAR();
        UNROLL for (int i = 8; i < 32; ++i) { gr[i] = ldrow(gp, lo, i); SCHED(); }
        sig(fb, F_GI);
        __builtin_amdgcn_s_setprio(1);
        f32x2 e[32][2];
        f32x2 p0 = {1.f, 1.f}, p1 = {1.f, 1.f};
        UNROLL for (int i = 0; i < 32; ++i) {
            f32x2 e0 = unpk(gr[i][0]) * NLOG2E, e1 = unpk(gr[i][1]) * NLOG2E;
            e0.x = __builtin_amdgcn_exp2f(e0.x);
            e0.y = __builtin_amdgcn_exp2f(e0.y);
            e1.x = __builtin_amdgcn_exp2f(e1.x);
            e1.y = __builtin_amdgcn_exp2f(e1.y);
            p0 = fma2(p0, e0, p0);
            p1 = fma2(p1, e1, p1);
            e[i][0] = e0;
            e[i][1] = e1;
            SCHED();
        }
        const u64 tag = mix_tag(((u64)(u32)__builtin_amdgcn_readfirstlane((u32)(nn >> 32)) << 32) | (u32)__builtin_amdgcn_readfirstlane((u32)nn));
        const u32 tag32 = (u32)tag;
        LAS char* decw = sm + L_DEC + ((l5 & 1) * 16 + ((l5 >> 1) & 3) * 4) * 16 + (l5 >> 3) * 4;
        {
            const u32 one = 0x3f800000u;
            f32x2 E0, E1;
            E0.x = __uint_as_float(__builtin_amdgcn_permlane32_swap(one, __float_as_uint(p0.x), false, false)[0]);
            E0.y = __uint_as_float(__builtin_amdgcn_permlane32_swap(one, __float_as_uint(p0.y), false, false)[0]);
            E1.x = __uint_as_float(__builtin_amdgcn_permlane32_swap(one, __float_as_uint(p1.x), false, false)[0]);
            E1.y = __uint_as_float(__builtin_amdgcn_permlane32_swap(one, __float_as_uint(p1.y), false, false)[0]);
            E0 *= C0;
            E1 *= C0;
            p0 *= E0;
            p1 *= E1;
            if (hh) {
                *(LAS float*)(decw + c * 512) = C0 * __builtin_amdgcn_rcpf(p0.x);
                *(LAS float*)(decw + c * 512 + 16) = C0 * __builtin_amdgcn_rcpf(p0.y);
                *(LAS float*)(decw + c * 512 + 32) = C0 * __builtin_amdgcn_rcpf(p1.x);
                *(LAS float*)(decw + c * 512 + 48) = C0 * __builtin_amdgcn_rcpf(p1.y);
            }
            bool bad = !(p0.x <= PLIM && p0.y <= PLIM && p1.x <= PLIM && p1.y <= PLIM);
            u32 anybad = __ballot(bad) != 0;
            if (lane == 0) *((LAS u32*)(sm + L_FLG) + F_BAD + c) = anybad;
            if (c == 0 && lane == 0) *(LAS u64*)(sm + L_TAG) = tag;
            sig(fb, F_GD);
            p0 = E0;
            p1 = E1;
        }
        float R[32][4];
        UNROLL for (int i = 0; i < 32; ++i) {
            p0 = fma2(p0, e[i][0], p0);
            p1 = fma2(p1, e[i][1], p1);
            PIN(p0); PIN(p1);
            R[i][0] = p0.x; R[i][1] = p0.y; R[i][2] = p1.x; R[i][3] = p1.y;
        }
        u32 himask = 0xffff0000u;
        PIN(himask);
#define SCALE2(i, x, oa, ob) do { u32 t0, t1, t2, t3; \
            asm("v_lshlrev_b32 %0, 16, %10\n\tv_and_b32 %1, %12, %10\n\tv_lshlrev_b32 %2, 16, %11\n\tv_and_b32 %3, %12, %11\n\t" \
                "v_mul_f32 %0, %0, %6\n\tv_mul_f32 %1, %1, %7\n\tv_mul_f32 %2, %2, %8\n\tv_mul_f32 %3, %3, %9\n\t" \
                "v_cvt_pk_bf16_f32 %4, %0, %2\n\tv_cvt_pk_bf16_f32 %5, %1, %3" \
                : "=&v"(t0), "=&v"(t1), "=&v"(t2), "=&v"(t3), "=v"(oa), "=v"(ob) \
                : "v"(R[i][2 * (x)]), "v"(R[i][2 * (x) + 1]), "v"(R[(i) + 1][2 * (x)]), "v"(R[(i) + 1][2 * (x) + 1]), \
                  "v"(raw[i][x]), "v"(raw[(i) + 1][x]), "v"(himask)); } while (0)
#define SCALE2K(i, x, oa, ob) do { u32 t0, t1, t2, t3; \
            asm("v_lshlrev_b32 %0, 16, %10\n\tv_and_b32 %1, %12, %10\n\tv_lshlrev_b32 %2, 16, %11\n\tv_and_b32 %3, %12, %11\n\t" \
                "v_mul_f32 %6, %0, %6\n\tv_mul_f32 %7, %1, %7\n\tv_mul_f32 %8, %2, %8\n\tv_mul_f32 %9, %3, %9\n\t" \
                "v_cvt_pk_bf16_f32 %4, %6, %8\n\tv_cvt_pk_bf16_f32 %5, %7, %9" \
                : "=&v"(t0), "=&v"(t1), "=&v"(t2), "=&v"(t3), "=&v"(oa), "=&v"(ob), \
                  "+v"(R[i][2 * (x)]), "+v"(R[i][2 * (x) + 1]), "+v"(R[(i) + 1][2 * (x)]), "+v"(R[(i) + 1][2 * (x) + 1]) \
                : "v"(raw[i][x]), "v"(raw[(i) + 1][x]), "v"(himask)); } while (0)
        wait_ge(fb, F_KL + c, 4);
        UNROLL for (int i = 0; i < 32; ++i) raw[i] = *(LAS u32x2*)(sm + L_KT + c * 16384 + lo + i * 256);
        MEMFENCE();
        u32 wbase = l5 * 512 + (((4 * hh) ^ (l5 & 7)) << 4);
        UNROLL for (int mm = 0; mm < 4; ++mm) {
            u32x4 o4[4];
            UNROLL for (int ii = 0; ii < 4; ++ii)
                UNROLL for (int x = 0; x < 2; ++x) { u32 oa, ob; SCALE2(8 * mm + 2 * ii, x, oa, ob); o4[2 * x][ii] = oa; o4[2 * x + 1][ii] = ob; }
            LAS char* d0 = sm + L_KT + c * 16384 + (wbase ^ (mm << 4));
            LAS char* d1 = sm + L_KT + c * 16384 + (wbase ^ (mm << 4) ^ 64);
            *(LAS u32x4*)(d0) = o4[0];
            *(LAS u32x4*)(d0 + 128) = o4[1];
            *(LAS u32x4*)(d1 + 256) = o4[2];
            *(LAS u32x4*)(d1 + 384) = o4[3];
            SCHED();
        }
        sig(fb, F_KR + c);
        wait_ge(fb, F_GD, 4);
        if (c == 0 && j < 7) {
            const u32 lane = lane_id();
            const u32 r16 = lane >> 2;
            LAS float* dp = (LAS float*)(sm + L_DEC) + (((r16 >> 2) & 1) * 16 + (r16 >> 3) * 4 + (r16 & 3)) * 4 + (lane & 3);
            float d0 = (dp[0] * dp[128]) * (dp[256] * dp[384]);
            float d1 = (dp[32] * dp[160]) * (dp[288] * dp[416]);
            GAS u64* gq = (GAS u64*)(ws + WS_D + (size_t)(head * 8 + j) * 1024) + lane * 2;
            __hip_atomic_store(gq, ((u64)tag32 << 32) | __float_as_uint(d0), RLX_AGENT);
            __hip_atomic_store(gq + 1, ((u64)tag32 << 32) | __float_as_uint(d1), RLX_AGENT);
        }
        if (any_bad(sm)) goto slow_path;
        {
        if (c == 3 && j > 0) {
            const __amdgpu_buffer_rsrc_t drs = __builtin_amdgcn_make_buffer_rsrc((void*)(ws_ + WS_D + (size_t)head * 8192), 0, 8192, 0x00020000);
            const u32 lane = lane_id();
            u32x4 dg[7];
            _Pragma("nounroll") for (u32 spins = 0; spins < SPIN_CAP; ++spins) {
                bool ok = true;
                UNROLL for (int n = 1; n < 7; ++n) dg[n] = __builtin_amdgcn_raw_buffer_load_b128(drs, lane * 16 + (n < j ? j - n : 8) * 1024, 0, 16);
                UNROLL for (int n = 1; n < 7; ++n) ok &= n >= j || (dg[n][1] == tag32 && dg[n][3] == tag32);
                if (__all(ok)) break;
                __builtin_amdgcn_s_sleep(2);
            }
            f32x2 wv = {1.f, 1.f};
            *(LAS u32*)(sm + L_WB + lane * 4) = 0x3f803f80u;
            UNROLL for (int n = 1; n < 7; ++n) {
                wv *= (f32x2){__uint_as_float(dg[n][0]), __uint_as_float(dg[n][2])};
                *(LAS u32*)(sm + L_WB + n * 256 + lane * 4) = pk(wv.x, wv.y);
            }
            sig(fb, F_WR);
        }
        {
            float sc = SCALE;
            PIN(sc);
            UNROLL for (int i = 0; i < 32; ++i)
                asm volatile("v_rcp_f32 %0, %0\n\tv_rcp_f32 %1, %1\n\tv_rcp_f32 %2, %2\n\tv_rcp_f32 %3, %3\n\t"
                             "v_mul_f32 %0, %0, %4\n\tv_mul_f32 %1, %1, %4\n\tv_mul_f32 %2, %2, %4\n\tv_mul_f32 %3, %3, %4"
                             : "+v"(R[i][0]), "+v"(R[i][1]), "+v"(R[i][2]), "+v"(R[i][3]) : "v"(sc));
        }
        if (c < 3) wait_ge(fb, F_VL + c, 4);
        else wait_ge(fb, F_VF, 4);
        PIN(lo);
        UNROLL for (int i = 0; i < 32; ++i) { raw[i] = ldrow(qp, lo, i); SCHED(); }
        PIN(wbase);
        {
            const u32 slotA = c == 0 ? L_X0 : L_V + (c == 2 ? 16384 : 0);
            const u32 need = c == 0 ? 0 : c == 3 ? 12 : 4;
            u32x4 o4[4][4];
            bool isfree = false;
#define QWRITE(mm) do { LAS char* d0 = sm + slotA + (wbase ^ ((mm) << 4)); LAS char* d1 = sm + slotA + (wbase ^ ((mm) << 4) ^ 64); \
                *(LAS u32x4*)(d0) = o4[mm][0]; *(LAS u32x4*)(d0 + 128) = o4[mm][1]; *(LAS u32x4*)(d1 + 256) = o4[mm][2]; *(LAS u32x4*)(d1 + 384) = o4[mm][3]; } while (0)
            UNROLL for (int mm = 0; mm < 4; ++mm) {
                u32 vfl = 0;
                UNROLL for (int ii = 0; ii < 4; ++ii) {
                    const int i = 8 * mm + 2 * ii;
                    if (ii == 3) { MEMFENCE(); vfl = FLAG(F_VF); MEMFENCE(); }
                    UNROLL for (int x = 0; x < 2; ++x) { u32 oa, ob; SCALE2K(i, x, oa, ob); o4[mm][2 * x][ii] = oa; o4[mm][2 * x + 1][ii] = ob; }
                    SCHED();
                }
                PIN(o4[mm][0]); PIN(o4[mm][1]); PIN(o4[mm][2]); PIN(o4[mm][3]);
                if (!isfree) {
                    isfree = (u32)__builtin_amdgcn_readfirstlane(vfl) >= need;
                    if (mm == 3 && !isfree) { wait_ge(fb, F_VF, need); isfree = true; }
                    if (isfree) { PIN(wbase); UNROLL for (int m2 = 0; m2 < mm; ++m2) QWRITE(m2); }
                }
                if (isfree) { PIN(wbase); QWRITE(mm); }
                SCHED();
            }
        }
        sig(fb, F_QR + c);
        if (j > 0) {
            if (c > 0) {
                const u32 slotB = c == 2 ? L_V + 32768 : L_KT + (c - 1) * 16384;
                float Fv[4] = {1.f, 1.f, 1.f, 1.f};
                {
                    PIN(lo);
                    const u32 l5b = (lo >> 3) & 31;
                    LAS char* decq = sm + L_DEC + ((l5b & 1) * 16 + ((l5b >> 1) & 3) * 4) * 16 + (l5b >> 3) * 4;
                    UNROLL for (int cc = 0; cc < 3; ++cc)
                        UNROLL for (int x = 0; x < 4; ++x) { const float dd = *(LAS float*)(decq + cc * 512 + x * 16); Fv[x] *= cc < c ? dd : 1.f; }
                }
                PIN(wbase);
                UNROLL for (int mm = 0; mm < 4; ++mm) {
                    u32x4 o4[4];
                    UNROLL for (int ii = 0; ii < 4; ++ii) {
                        const int i = 8 * mm + 2 * ii;
                        UNROLL for (int x = 0; x < 2; ++x) {
                            u32 t0, t1, oa, ob;
                            asm("v_mul_f32 %0, %4, %8\n\tv_mul_f32 %1, %5, %9\n\tv_mul_f32 %2, %6, %8\n\tv_mul_f32 %3, %7, %9\n\t"
                                "v_cvt_pk_bf16_f32 %0, %0, %2\n\tv_cvt_pk_bf16_f32 %1, %1, %3"
                                : "=&v"(oa), "=&v"(ob), "=&v"(t0), "=&v"(t1)
                                : "v"(R[i][2 * x]), "v"(R[i][2 * x + 1]), "v"(R[i + 1][2 * x]), "v"(R[i + 1][2 * x + 1]), "v"(Fv[2 * x]), "v"(Fv[2 * x + 1]));
                            o4[2 * x][ii] = oa;
                            o4[2 * x + 1][ii] = ob;
                        }
                    }
                    if (mm == 0) wait_ge(fb, c == 2 ? F_VF : F_AT + c - 1, c == 2 ? 8 : 4);
                    LAS char* d0 = sm + slotB + (wbase ^ (mm << 4));
                    LAS char* d1 = sm + slotB + (wbase ^ (mm << 4) ^ 64);
                    *(LAS u32x4*)(d0) = o4[0];
                    *(LAS u32x4*)(d0 + 128) = o4[1];
                    *(LAS u32x4*)(d1 + 256) = o4[2];
                    *(LAS u32x4*)(d1 + 384) = o4[3];
                    SCHED();
                }
                if (c == 3) sig(fb, F_QB);
            }
            if (c < 3) {
                const u32 lane = lane_id();
                const u32 g4 = lane >> 4, r16 = lane & 15;
                const u32 smb = (u32)(uintptr_t)sm;
                const u32 lp = ((g4 & 1) * 32 + r16) * 16;
                u32 sel[4];
                {
                    const bool act = ((r16 >> 2) & 1) == (g4 & 1);
                    const u32 dw = 2 * (r16 >> 3) + ((r16 >> 1) & 1);
                    UNROLL for (int x = 0; x < 4; ++x) sel[x] = (act && dw == (u32)x) ? ((r16 & 1) ? 0x01000c0cu : 0x0c0c0100u) : 0x0c0c0c0cu;
                }
                const u64 sbase = (u64)(uintptr_t)(ws_ + WS_S + (size_t)head * 8 * 32768);
                const u32x4 srs = {(u32)sbase, (u32)(sbase >> 32) & 0xffffu, 8u * 32768u, 0x00020000u};
                wait_ge(fb, F_WR, 1);
                __builtin_amdgcn_s_setprio(3);
                asm volatile(
                    "s_mov_b32 s40, %[full]\n"
                    "s_mov_b32 s46, %[full]\n"
                    "s_mov_b32 s47, 0\n"
                    "s_mov_b32 s41, 0x40000\n"
                    "s_mov_b64 exec, s[46:47]\n"
                    "global_load_dwordx2 v[44:45], %[fo], %[fl] sc1\n"
                    "s_mov_b64 exec, -1\n"
                    "s_add_u32 s50, %[g0], 0\n"
                    "s_add_u32 s51, %[g0], 2048\n"
                    "s_add_u32 s52, %[g0], 4096\n"
                    "s_add_u32 s53, %[g0], 6144\n"
                    "s_add_u32 s54, %[g0], 8192\n"
                    "s_add_u32 s55, %[g0], 10240\n"
                    "s_lshr_b32 s43, %[g0], 11\n"
                    "v_add_u32 v18, s43, %[wl]\n"
                    "v_mov_b32 v48, 0\n"
                    "v_mov_b32 v49, 0\n"
                    "v_mov_b32 v50, 0\n"
                    "v_mov_b32 v51, 0\n"
                    "v_mov_b32 v52, 0\n"
                    "v_mov_b32 v53, 0\n"
                    "v_mov_b32 v54, 0\n"
                    "v_mov_b32 v55, 0\n"
                    "v_mov_b32 v56, 0\n"
                    "v_mov_b32 v57, 0\n"
                    "v_mov_b32 v58, 0\n"
                    "v_mov_b32 v59, 0\n"
                    "v_mov_b32 v60, 0\n"
                    "v_mov_b32 v61, 0\n"
                    "v_mov_b32 v62, 0\n"
                    "v_mov_b32 v63, 0\n"
                    "v_mov_b32 v64, 0\n"
                    "v_mov_b32 v65, 0\n"
                    "v_mov_b32 v66, 0\n"
                    "v_mov_b32 v67, 0\n"
                    "v_mov_b32 v68, 0\n"
                    "v_mov_b32 v69, 0\n"
                    "v_mov_b32 v70, 0\n"
                    "v_mov_b32 v71, 0\n"
                    "v_mov_b32 v72, 0\n"
                    "v_mov_b32 v73, 0\n"
                    "v_mov_b32 v74, 0\n"
                    "v_mov_b32 v75, 0\n"
                    "v_mov_b32 v76, 0\n"
                    "v_mov_b32 v77, 0\n"
                    "v_mov_b32 v78, 0\n"
                    "v_mov_b32 v79, 0\n"
                    "v_mov_b32 v80, 0\n"
                    "v_mov_b32 v81, 0\n"
                    "v_mov_b32 v82, 0\n"
                    "v_mov_b32 v83, 0\n"
                    "v_mov_b32 v84, 0\n"
                    "v_mov_b32 v85, 0\n"
                    "v_mov_b32 v86, 0\n"
                    "v_mov_b32 v87, 0\n"
                    "v_mov_b32 v88, 0\n"
                    "v_mov_b32 v89, 0\n"
                    "v_mov_b32 v90, 0\n"
                    "v_mov_b32 v91, 0\n"
                    "v_mov_b32 v92, 0\n"
                    "v_mov_b32 v93, 0\n"
                    "v_mov_b32 v94, 0\n"
                    "v_mov_b32 v95, 0\n"
                    "v_mov_b32 v96, 0\n"
                    "v_mov_b32 v97, 0\n"
                    "v_mov_b32 v98, 0\n"
                    "v_mov_b32 v99, 0\n"
                    "v_mov_b32 v100, 0\n"
                    "v_mov_b32 v101, 0\n"
                    "v_mov_b32 v102, 0\n"
                    "v_mov_b32 v103, 0\n"
                    "v_mov_b32 v104, 0\n"
                    "v_mov_b32 v105, 0\n"
                    "v_mov_b32 v106, 0\n"
                    "v_mov_b32 v107, 0\n"
                    "v_mov_b32 v108, 0\n"
                    "v_mov_b32 v109, 0\n"
                    "v_mov_b32 v110, 0\n"
                    "v_mov_b32 v111, 0\n"
                    "v_mov_b32 v112, 0\n"
                    "v_mov_b32 v113, 0\n"
                    "v_mov_b32 v114, 0\n"
                    "v_mov_b32 v115, 0\n"
                    "v_mov_b32 v116, 0\n"
                    "v_mov_b32 v117, 0\n"
                    "v_mov_b32 v118, 0\n"
                    "v_mov_b32 v119, 0\n"
                    "v_mov_b32 v120, 0\n"
                    "v_mov_b32 v121, 0\n"
                    "v_mov_b32 v122, 0\n"
                    "v_mov_b32 v123, 0\n"
                    "v_mov_b32 v124, 0\n"
                    "v_mov_b32 v125, 0\n"
                    "v_mov_b32 v126, 0\n"
                    "v_mov_b32 v127, 0\n"
                    "v_mov_b32 v128, 0\n"
                    "v_mov_b32 v129, 0\n"
                    "v_mov_b32 v130, 0\n"
                    "v_mov_b32 v131, 0\n"
                    "v_mov_b32 v132, 0\n"
                    "v_mov_b32 v133, 0\n"
                    "v_mov_b32 v134, 0\n"
                    "v_mov_b32 v135, 0\n"
                    "v_mov_b32 v136, 0\n"
                    "v_mov_b32 v137, 0\n"
                    "v_mov_b32 v138, 0\n"
                    "v_mov_b32 v139, 0\n"
                    "v_mov_b32 v140, 0\n"
                    "v_mov_b32 v141, 0\n"
                    "v_mov_b32 v142, 0\n"
                    "v_mov_b32 v143, 0\n"
                    "s_cmp_eq_u32 %[g0], 0x6000\n"
                    "s_cbranch_scc1 .Lg2_first4\n"
                    "s_branch .Lg2_first6\n"
                    ".Lg2_spin6:\n"
                    "s_sleep 1\n"
                    "s_sub_u32 s41, s41, 1\n"
                    "s_cbranch_scc1 .Lg2_last6\n"
                    "s_mov_b64 exec, s[46:47]\n"
                    "global_load_dwordx2 v[44:45], %[fo], %[fl] sc1\n"
                    "s_mov_b64 exec, -1\n"
                    ".Lg2_first6:\n"
                    "s_waitcnt vmcnt(0)\n"
                    "v_cmp_eq_u64 s[42:43], %[tag], v[44:45]\n"
                    "s_nop 3\n"
                    "s_and_b32 s42, s42, s40\n"
                    "s_cbranch_scc0 .Lg2_spin6\n"
                    "s_ff1_i32_b32 s44, s42\n"
                    "s_bitset0_b32 s42, s44\n"
                    "s_ff1_i32_b32 s45, s42\n"
                    "s_cmp_lt_i32 s45, 0\n"
                    "s_cbranch_scc0 1f\n"
                    "s_bcnt1_i32_b32 s43, s40\n"
                    "s_cmp_eq_u32 s43, 1\n"
                    "s_cbranch_scc0 .Lg2_spin6\n"
                    "s_mov_b32 s48, 0x80000000\n"
                    "s_mov_b32 s49, 0\n"
                    "s_branch 2f\n"
                    "1:\n"
                    "s_bitset0_b32 s40, s45\n"
                    "s_lshl_b32 s48, s45, 15\n"
                    "s_sub_u32 s49, %[jm], s45\n"
                    "s_lshl_b32 s49, s49, 8\n"
                    "2:\n"
                    "s_bitset0_b32 s40, s44\n"
                    "s_lshl_b32 s43, s44, 15\n"
                    "s_sub_u32 s44, %[jm], s44\n"
                    "s_lshl_b32 s44, s44, 8\n"
                    "v_add_u32 v16, s43, %[lp]\n"
                    "v_add_u32 v17, s44, v18\n"
                    "s_mov_b32 exec_lo, 0\n"
                    "v_add_u32 v16, s48, %[lp]\n"
                    "v_add_u32 v17, s49, v18\n"
                    "s_mov_b32 exec_lo, -1\n"
                    "s_mov_b64 exec, s[46:47]\n"
                    "global_load_dwordx2 v[44:45], %[fo], %[fl] sc1\n"
                    "s_mov_b64 exec, -1\n"
                    "buffer_load_dwordx4 v[144:147], v16, %[srs], s50 offen offset:0 sc1\n"
                    "buffer_load_dwordx4 v[148:151], v16, %[srs], s50 offen offset:256 sc1\n"
                    "buffer_load_dwordx4 v[152:155], v16, %[srs], s50 offen offset:1024 sc1\n"
                    "buffer_load_dwordx4 v[156:159], v16, %[srs], s50 offen offset:1280 sc1\n"
                    "ds_read_u16 v20, v17 offset:0\n"
                    "buffer_load_dwordx4 v[160:163], v16, %[srs], s51 offen offset:0 sc1\n"
                    "buffer_load_dwordx4 v[164:167], v16, %[srs], s51 offen offset:256 sc1\n"
                    "buffer_load_dwordx4 v[168:171], v16, %[srs], s51 offen offset:1024 sc1\n"
                    "buffer_load_dwordx4 v[172:175], v16, %[srs], s51 offen offset:1280 sc1\n"
                    "ds_read_u16 v21, v17 offset:0\n"
                    "buffer_load_dwordx4 v[176:179], v16, %[srs], s52 offen offset:0 sc1\n"
                    "buffer_load_dwordx4 v[180:183], v16, %[srs], s52 offen offset:256 sc1\n"
                    "buffer_load_dwordx4 v[184:187], v16, %[srs], s52 offen offset:1024 sc1\n"
                    "buffer_load_dwordx4 v[188:191], v16, %[srs], s52 offen offset:1280 sc1\n"
                    "ds_read_u16 v22, v17 offset:2\n"
                    "buffer_load_dwordx4 v[192:195], v16, %[srs], s53 offen offset:0 sc1\n"
                    "buffer_load_dwordx4 v[196:199], v16, %[srs], s53 offen offset:256 sc1\n"
                    "buffer_load_dwordx4 v[200:203], v16, %[srs], s53 offen offset:1024 sc1\n"
                    "buffer_load_dwordx4 v[204:207], v16, %[srs], s53 offen offset:1280 sc1\n"
                    "ds_read_u16 v23, v17 offset:2\n"
                    "buffer_load_dwordx4 v[208:211], v16, %[srs], s54 offen offset:0 sc1\n"
                    "buffer_load_dwordx4 v[212:215], v16, %[srs], s54 offen offset:256 sc1\n"
                    "buffer_load_dwordx4 v[216:219], v16, %[srs], s54 offen offset:1024 sc1\n"
                    "buffer_load_dwordx4 v[220:223], v16, %[srs], s54 offen offset:1280 sc1\n"
                    "ds_read_u16 v24, v17 offset:4\n"
                    "buffer_load_dwordx4 v[224:227], v16, %[srs], s55 offen offset:0 sc1\n"
                    "buffer_load_dwordx4 v[228:231], v16, %[srs], s55 offen offset:256 sc1\n"
                    "buffer_load_dwordx4 v[232:235], v16, %[srs], s55 offen offset:1024 sc1\n"
                    "buffer_load_dwordx4 v[236:239], v16, %[srs], s55 offen offset:1280 sc1\n"
                    "ds_read_u16 v25, v17 offset:4\n"
                    ".Lg2_loop6:\n"
                    "s_cmp_eq_u32 s40, 0\n"
                    "s_cbranch_scc1 .Lg2_last6\n"
                    "s_waitcnt vmcnt(24) lgkmcnt(0)\n"
                    "v_cmp_eq_u64 s[42:43], %[tag], v[44:45]\n"
                    "s_nop 3\n"
                    "s_and_b32 s42, s42, s40\n"
                    "s_cbranch_scc0 .Lg2_drain6\n"
                    "s_ff1_i32_b32 s44, s42\n"
                    "s_bitset0_b32 s42, s44\n"
                    "s_ff1_i32_b32 s45, s42\n"
                    "s_cmp_lt_i32 s45, 0\n"
                    "s_cbranch_scc0 1f\n"
                    "s_bcnt1_i32_b32 s43, s40\n"
                    "s_cmp_eq_u32 s43, 1\n"
                    "s_cbranch_scc0 .Lg2_drain6\n"
                    "s_mov_b32 s48, 0x80000000\n"
                    "s_mov_b32 s49, 0\n"
                    "s_branch 2f\n"
                    "1:\n"
                    "s_bitset0_b32 s40, s45\n"
                    "s_lshl_b32 s48, s45, 15\n"
                    "s_sub_u32 s49, %[jm], s45\n"
                    "s_lshl_b32 s49, s49, 8\n"
                    "2:\n"
                    "s_bitset0_b32 s40, s44\n"
                    "s_lshl_b32 s43, s44, 15\n"
                    "s_sub_u32 s44, %[jm], s44\n"
                    "s_lshl_b32 s44, s44, 8\n"
                    "v_add_u32 v16, s43, %[lp]\n"
                    "v_add_u32 v17, s44, v18\n"
                    "s_mov_b32 exec_lo, 0\n"
                    "v_add_u32 v16, s48, %[lp]\n"
                    "v_add_u32 v17, s49, v18\n"
                    "s_mov_b32 exec_lo, -1\n"
                    "s_mov_b64 exec, s[46:47]\n"
                    "global_load_dwordx2 v[44:45], %[fo], %[fl] sc1\n"
                    "s_mov_b64 exec, -1\n"
                    "v_perm_b32 v40, v20, v20, %[e0]\n"
                    "v_perm_b32 v41, v20, v20, %[e1]\n"
                    "v_perm_b32 v42, v20, v20, %[e2]\n"
                    "v_perm_b32 v43, v20, v20, %[e3]\n"
                    "s_waitcnt vmcnt(21)\n"
                    "s_nop 0\n"
                    "v_mfma_f32_16x16x32_bf16 v[48:51], v[40:43], v[144:147], v[48:51]\n"
                    "v_mfma_f32_16x16x32_bf16 v[52:55], v[40:43], v[148:151], v[52:55]\n"
                    "buffer_load_dwordx4 v[144:147], v16, %[srs], s50 offen offset:0 sc1\n"
                    "v_mfma_f32_16x16x32_bf16 v[56:59], v[40:43], v[152:155], v[56:59]\n"
                    "buffer_load_dwordx4 v[148:151], v16, %[srs], s50 offen offset:256 sc1\n"
                    "v_mfma_f32_16x16x32_bf16 v[60:63], v[40:43], v[156:159], v[60:63]\n"
                    "buffer_load_dwordx4 v[152:155], v16, %[srs], s50 offen offset:1024 sc1\n"
                    "buffer_load_dwordx4 v[156:159], v16, %[srs], s50 offen offset:1280 sc1\n"
                    "ds_read_u16 v20, v17 offset:0\n"
                    "v_perm_b32 v40, v21, v21, %[e0]\n"
                    "v_perm_b32 v41, v21, v21, %[e1]\n"
                    "v_perm_b32 v42, v21, v21, %[e2]\n"
                    "v_perm_b32 v43, v21, v21, %[e3]\n"
                    "s_waitcnt vmcnt(21)\n"
                    "s_nop 0\n"
                    "v_mfma_f32_16x16x32_bf16 v[64:67], v[40:43], v[160:163], v[64:67]\n"
                    "v_mfma_f32_16x16x32_bf16 v[68:71], v[40:43], v[164:167], v[68:71]\n"
                    "buffer_load_dwordx4 v[160:163], v16, %[srs], s51 offen offset:0 sc1\n"
                    "v_mfma_f32_16x16x32_bf16 v[72:75], v[40:43], v[168:171], v[72:75]\n"
                    "buffer_load_dwordx4 v[164:167], v16, %[srs], s51 offen offset:256 sc1\n"
                    "v_mfma_f32_16x16x32_bf16 v[76:79], v[40:43], v[172:175], v[76:79]\n"
                    "buffer_load_dwordx4 v[168:171], v16, %[srs], s51 offen offset:1024 sc1\n"
                    "buffer_load_dwordx4 v[172:175], v16, %[srs], s51 offen offset:1280 sc1\n"
                    "ds_read_u16 v21, v17 offset:0\n"
                    "v_perm_b32 v40, v22, v22, %[e0]\n"
                    "v_perm_b32 v41, v22, v22, %[e1]\n"
                    "v_perm_b32 v42, v22, v22, %[e2]\n"
                    "v_perm_b32 v43, v22, v22, %[e3]\n"
                    "s_waitcnt vmcnt(21)\n"
                    "s_nop 0\n"
                    "v_mfma_f32_16x16x32_bf16 v[80:83], v[40:43], v[176:179], v[80:83]\n"
                    "v_mfma_f32_16x16x32_bf16 v[84:87], v[40:43], v[180:183], v[84:87]\n"
                    "buffer_load_dwordx4 v[176:179], v16, %[srs], s52 offen offset:0 sc1\n"
                    "v_mfma_f32_16x16x32_bf16 v[88:91], v[40:43], v[184:187], v[88:91]\n"
                    "buffer_load_dwordx4 v[180:183], v16, %[srs], s52 offen offset:256 sc1\n"
                    "v_mfma_f32_16x16x32_bf16 v[92:95], v[40:43], v[188:191], v[92:95]\n"
                    "buffer_load_dwordx4 v[184:187], v16, %[srs], s52 offen offset:1024 sc1\n"
                    "buffer_load_dwordx4 v[188:191], v16, %[srs], s52 offen offset:1280 sc1\n"
                    "ds_read_u16 v22, v17 offset:2\n"
                    "v_perm_b32 v40, v23, v23, %[e0]\n"
                    "v_perm_b32 v41, v23, v23, %[e1]\n"
                    "v_perm_b32 v42, v23, v23, %[e2]\n"
                    "v_perm_b32 v43, v23, v23, %[e3]\n"
                    "s_waitcnt vmcnt(21)\n"
                    "s_nop 0\n"
                    "v_mfma_f32_16x16x32_bf16 v[96:99], v[40:43], v[192:195], v[96:99]\n"
                    "v_mfma_f32_16x16x32_bf16 v[100:103], v[40:43], v[196:199], v[100:103]\n"
                    "buffer_load_dwordx4 v[192:195], v16, %[srs], s53 offen offset:0 sc1\n"
                    "v_mfma_f32_16x16x32_bf16 v[104:107], v[40:43], v[200:203], v[104:107]\n"
                    "buffer_load_dwordx4 v[196:199], v16, %[srs], s53 offen offset:256 sc1\n"
                    "v_mfma_f32_16x16x32_bf16 v[108:111], v[40:43], v[204:207], v[108:111]\n"
                    "buffer_load_dwordx4 v[200:203], v16, %[srs], s53 offen offset:1024 sc1\n"
                    "buffer_load_dwordx4 v[204:207], v16, %[srs], s53 offen offset:1280 sc1\n"
                    "ds_read_u16 v23, v17 offset:2\n"
                    "v_perm_b32 v40, v24, v24, %[e0]\n"
                    "v_perm_b32 v41, v24, v24, %[e1]\n"
                    "v_perm_b32 v42, v24, v24, %[e2]\n"
                    "v_perm_b32 v43, v24, v24, %[e3]\n"
                    "s_waitcnt vmcnt(21)\n"
                    "s_nop 0\n"
                    "v_mfma_f32_16x16x32_bf16 v[112:115], v[40:43], v[208:211], v[112:115]\n"
                    "v_mfma_f32_16x16x32_bf16 v[116:119], v[40:43], v[212:215], v[116:119]\n"
                    "buffer_load_dwordx4 v[208:211], v16, %[srs], s54 offen offset:0 sc1\n"
                    "v_mfma_f32_16x16x32_bf16 v[120:123], v[40:43], v[216:219], v[120:123]\n"
                    "buffer_load_dwordx4 v[212:215], v16, %[srs], s54 offen offset:256 sc1\n"
                    "v_mfma_f32_16x16x32_bf16 v[124:127], v[40:43], v[220:223], v[124:127]\n"
                    "buffer_load_dwordx4 v[216:219], v16, %[srs], s54 offen offset:1024 sc1\n"
                    "buffer_load_dwordx4 v[220:223], v16, %[srs], s54 offen offset:1280 sc1\n"
                    "ds_read_u16 v24, v17 offset:4\n"
                    "v_perm_b32 v40, v25, v25, %[e0]\n"
                    "v_perm_b32 v41, v25, v25, %[e1]\n"
                    "v_perm_b32 v42, v25, v25, %[e2]\n"
                    "v_perm_b32 v43, v25, v25, %[e3]\n"
                    "s_waitcnt vmcnt(21)\n"
                    "s_nop 0\n"
                    "v_mfma_f32_16x16x32_bf16 v[128:131], v[40:43], v[224:227], v[128:131]\n"
                    "v_mfma_f32_16x16x32_bf16 v[132:135], v[40:43], v[228:231], v[132:135]\n"
                    "buffer_load_dwordx4 v[224:227], v16, %[srs], s55 offen offset:0 sc1\n"
                    "v_mfma_f32_16x16x32_bf16 v[136:139], v[40:43], v[232:235], v[136:139]\n"
                    "buffer_load_dwordx4 v[228:231], v16, %[srs], s55 offen offset:256 sc1\n"
                    "v_mfma_f32_16x16x32_bf16 v[140:143], v[40:43], v[236:239], v[140:143]\n"
                    "buffer_load_dwordx4 v[232:235], v16, %[srs], s55 offen offset:1024 sc1\n"
                    "buffer_load_dwordx4 v[236:239], v16, %[srs], s55 offen offset:1280 sc1\n"
                    "ds_read_u16 v25, v17 offset:4\n"
                    "s_branch .Lg2_loop6\n"
                    ".Lg2_drain6:\n"
                    "s_mov_b64 exec, s[46:47]\n"
                    "global_load_dwordx2 v[44:45], %[fo], %[fl] sc1\n"
                    "s_mov_b64 exec, -1\n"
                    "v_perm_b32 v40, v20, v20, %[e0]\n"
                    "v_perm_b32 v41, v20, v20, %[e1]\n"
                    "v_perm_b32 v42, v20, v20, %[e2]\n"
                    "v_perm_b32 v43, v20, v20, %[e3]\n"
                    "s_waitcnt vmcnt(21)\n"
                    "s_nop 0\n"
                    "v_mfma_f32_16x16x32_bf16 v[48:51], v[40:43], v[144:147], v[48:51]\n"
                    "v_mfma_f32_16x16x32_bf16 v[52:55], v[40:43], v[148:151], v[52:55]\n"
                    "v_mfma_f32_16x16x32_bf16 v[56:59], v[40:43], v[152:155], v[56:59]\n"
                    "v_mfma_f32_16x16x32_bf16 v[60:63], v[40:43], v[156:159], v[60:63]\n"
                    "v_perm_b32 v40, v21, v21, %[e0]\n"
                    "v_perm_b32 v41, v21, v21, %[e1]\n"
                    "v_perm_b32 v42, v21, v21, %[e2]\n"
                    "v_perm_b32 v43, v21, v21, %[e3]\n"
                    "s_waitcnt vmcnt(17)\n"
                    "s_nop 0\n"
                    "v_mfma_f32_16x16x32_bf16 v[64:67], v[40:43], v[160:163], v[64:67]\n"
                    "v_mfma_f32_16x16x32_bf16 v[68:71], v[40:43], v[164:167], v[68:71]\n"
                    "v_mfma_f32_16x16x32_bf16 v[72:75], v[40:43], v[168:171], v[72:75]\n"
                    "v_mfma_f32_16x16x32_bf16 v[76:79], v[40:43], v[172:175], v[76:79]\n"
                    "v_perm_b32 v40, v22, v22, %[e0]\n"
                    "v_perm_b32 v41, v22, v22, %[e1]\n"
                    "v_perm_b32 v42, v22, v22, %[e2]\n"
                    "v_perm_b32 v43, v22, v22, %[e3]\n"
                    "s_waitcnt vmcnt(13)\n"
                    "s_nop 0\n"
                    "v_mfma_f32_16x16x32_bf16 v[80:83], v[40:43], v[176:179], v[80:83]\n"
                    "v_mfma_f32_16x16x32_bf16 v[84:87], v[40:43], v[180:183], v[84:87]\n"
                    "v_mfma_f32_16x16x32_bf16 v[88:91], v[40:43], v[184:187], v[88:91]\n"
                    "v_mfma_f32_16x16x32_bf16 v[92:95], v[40:43], v[188:191], v[92:95]\n"
                    "v_perm_b32 v40, v23, v23, %[e0]\n"
                    "v_perm_b32 v41, v23, v23, %[e1]\n"
                    "v_perm_b32 v42, v23, v23, %[e2]\n"
                    "v_perm_b32 v43, v23, v23, %[e3]\n"
                    "s_waitcnt vmcnt(9)\n"
                    "s_nop 0\n"
                    "v_mfma_f32_16x16x32_bf16 v[96:99], v[40:43], v[192:195], v[96:99]\n"
                    "v_mfma_f32_16x16x32_bf16 v[100:103], v[40:43], v[196:199], v[100:103]\n"
                    "v_mfma_f32_16x16x32_bf16 v[104:107], v[40:43], v[200:203], v[104:107]\n"
                    "v_mfma_f32_16x16x32_bf16 v[108:111], v[40:43], v[204:207], v[108:111]\n"
                    "v_perm_b32 v40, v24, v24, %[e0]\n"
                    "v_perm_b32 v41, v24, v24, %[e1]\n"
                    "v_perm_b32 v42, v24, v24, %[e2]\n"
                    "v_perm_b32 v43, v24, v24, %[e3]\n"
                    "s_waitcnt vmcnt(5)\n"
                    "s_nop 0\n"
                    "v_mfma_f32_16x16x32_bf16 v[112:115], v[40:43], v[208:211], v[112:115]\n"
                    "v_mfma_f32_16x16x32_bf16 v[116:119], v[40:43], v[212:215], v[116:119]\n"
                    "v_mfma_f32_16x16x32_bf16 v[120:123], v[40:43], v[216:219], v[120:123]\n"
                    "v_mfma_f32_16x16x32_bf16 v[124:127], v[40:43], v[220:223], v[124:127]\n"
                    "v_perm_b32 v40, v25, v25, %[e0]\n"
                    "v_perm_b32 v41, v25, v25, %[e1]\n"
                    "v_perm_b32 v42, v25, v25, %[e2]\n"
                    "v_perm_b32 v43, v25, v25, %[e3]\n"
                    "s_waitcnt vmcnt(1)\n"
                    "s_nop 0\n"
                    "v_mfma_f32_16x16x32_bf16 v[128:131], v[40:43], v[224:227], v[128:131]\n"
                    "v_mfma_f32_16x16x32_bf16 v[132:135], v[40:43], v[228:231], v[132:135]\n"
                    "v_mfma_f32_16x16x32_bf16 v[136:139], v[40:43], v[232:235], v[136:139]\n"
                    "v_mfma_f32_16x16x32_bf16 v[140:143], v[40:43], v[236:239], v[140:143]\n"
                    "s_branch .Lg2_first6\n"
                    ".Lg2_last6:\n"
                    "v_mov_b32 v46, %[vfa]\n"
                    "s_mov_b32 s43, 0x400000\n"
                    "3:\n"
                    "ds_read_b32 v47, v46\n"
                    "s_waitcnt lgkmcnt(0)\n"
                    "v_readfirstlane_b32 s44, v47\n"
                    "s_nop 0\n"
                    "s_cmp_ge_u32 s44, 12\n"
                    "s_cbranch_scc1 4f\n"
                    "s_sleep 1\n"
                    "s_sub_u32 s43, s43, 1\n"
                    "s_cbranch_scc0 3b\n"
                    "4:\n"
                    "v_perm_b32 v40, v20, v20, %[e0]\n"
                    "v_perm_b32 v41, v20, v20, %[e1]\n"
                    "v_perm_b32 v42, v20, v20, %[e2]\n"
                    "v_perm_b32 v43, v20, v20, %[e3]\n"
                    "s_waitcnt vmcnt(20)\n"
                    "s_nop 0\n"
                    "v_mfma_f32_16x16x32_bf16 v[48:51], v[40:43], v[144:147], v[48:51]\n"
                    "v_mfma_f32_16x16x32_bf16 v[52:55], v[40:43], v[148:151], v[52:55]\n"
                    "v_mfma_f32_16x16x32_bf16 v[56:59], v[40:43], v[152:155], v[56:59]\n"
                    "v_mfma_f32_16x16x32_bf16 v[60:63], v[40:43], v[156:159], v[60:63]\n"
                    "v_perm_b32 v40, v21, v21, %[e0]\n"
                    "v_perm_b32 v41, v21, v21, %[e1]\n"
                    "v_perm_b32 v42, v21, v21, %[e2]\n"
                    "v_perm_b32 v43, v21, v21, %[e3]\n"
                    "s_waitcnt vmcnt(16)\n"
                    "s_nop 0\n"
                    "v_mfma_f32_16x16x32_bf16 v[64:67], v[40:43], v[160:163], v[64:67]\n"
                    "v_mfma_f32_16x16x32_bf16 v[68:71], v[40:43], v[164:167], v[68:71]\n"
                    "v_mfma_f32_16x16x32_bf16 v[72:75], v[40:43], v[168:171], v[72:75]\n"
                    "v_mfma_f32_16x16x32_bf16 v[76:79], v[40:43], v[172:175], v[76:79]\n"
                    "v_cvt_pk_bf16_f32 v32, v48, v49\n"
                    "v_cvt_pk_bf16_f32 v33, v50, v51\n"
                    "v_cvt_pk_bf16_f32 v34, v52, v53\n"
                    "v_cvt_pk_bf16_f32 v35, v54, v55\n"
                    "v_cvt_pk_bf16_f32 v36, v56, v57\n"
                    "v_cvt_pk_bf16_f32 v37, v58, v59\n"
                    "v_cvt_pk_bf16_f32 v38, v60, v61\n"
                    "v_cvt_pk_bf16_f32 v39, v62, v63\n"
                    "s_cmp_lt_u32 s50, 0x4000\n"
                    "s_cselect_b32 s43, -81920, 0\n"
                    "s_add_u32 s43, s43, s50\n"
                    "v_add_u32 v46, s43, %[sda]\n"
                    "ds_write2_b64 v46, v[32:33], v[34:35] offset1:32\n"
                    "ds_write2_b64 v46, v[36:37], v[38:39] offset0:128 offset1:160\n"
                    "v_perm_b32 v40, v22, v22, %[e0]\n"
                    "v_perm_b32 v41, v22, v22, %[e1]\n"
                    "v_perm_b32 v42, v22, v22, %[e2]\n"
                    "v_perm_b32 v43, v22, v22, %[e3]\n"
                    "s_waitcnt vmcnt(12)\n"
                    "s_nop 0\n"
                    "v_mfma_f32_16x16x32_bf16 v[80:83], v[40:43], v[176:179], v[80:83]\n"
                    "v_mfma_f32_16x16x32_bf16 v[84:87], v[40:43], v[180:183], v[84:87]\n"
                    "v_mfma_f32_16x16x32_bf16 v[88:91], v[40:43], v[184:187], v[88:91]\n"
                    "v_mfma_f32_16x16x32_bf16 v[92:95], v[40:43], v[188:191], v[92:95]\n"
                    "v_cvt_pk_bf16_f32 v32, v64, v65\n"
                    "v_cvt_pk_bf16_f32 v33, v66, v67\n"
                    "v_cvt_pk_bf16_f32 v34, v68, v69\n"
                    "v_cvt_pk_bf16_f32 v35, v70, v71\n"
                    "v_cvt_pk_bf16_f32 v36, v72, v73\n"
                    "v_cvt_pk_bf16_f32 v37, v74, v75\n"
                    "v_cvt_pk_bf16_f32 v38, v76, v77\n"
                    "v_cvt_pk_bf16_f32 v39, v78, v79\n"
                    "s_cmp_lt_u32 s51, 0x4000\n"
                    "s_cselect_b32 s43, -81920, 0\n"
                    "s_add_u32 s43, s43, s51\n"
                    "v_add_u32 v46, s43, %[sda]\n"
                    "ds_write2_b64 v46, v[32:33], v[34:35] offset1:32\n"
                    "ds_write2_b64 v46, v[36:37], v[38:39] offset0:128 offset1:160\n"
                    "v_perm_b32 v40, v23, v23, %[e0]\n"
                    "v_perm_b32 v41, v23, v23, %[e1]\n"
                    "v_perm_b32 v42, v23, v23, %[e2]\n"
                    "v_perm_b32 v43, v23, v23, %[e3]\n"
                    "s_waitcnt vmcnt(8)\n"
                    "s_nop 0\n"
                    "v_mfma_f32_16x16x32_bf16 v[96:99], v[40:43], v[192:195], v[96:99]\n"
                    "v_mfma_f32_16x16x32_bf16 v[100:103], v[40:43], v[196:199], v[100:103]\n"
                    "v_mfma_f32_16x16x32_bf16 v[104:107], v[40:43], v[200:203], v[104:107]\n"
                    "v_mfma_f32_16x16x32_bf16 v[108:111], v[40:43], v[204:207], v[108:111]\n"
                    "v_cvt_pk_bf16_f32 v32, v80, v81\n"
                    "v_cvt_pk_bf16_f32 v33, v82, v83\n"
                    "v_cvt_pk_bf16_f32 v34, v84, v85\n"
                    "v_cvt_pk_bf16_f32 v35, v86, v87\n"
                    "v_cvt_pk_bf16_f32 v36, v88, v89\n"
                    "v_cvt_pk_bf16_f32 v37, v90, v91\n"
                    "v_cvt_pk_bf16_f32 v38, v92, v93\n"
                    "v_cvt_pk_bf16_f32 v39, v94, v95\n"
                    "s_cmp_lt_u32 s52, 0x4000\n"
                    "s_cselect_b32 s43, -81920, 0\n"
                    "s_add_u32 s43, s43, s52\n"
                    "v_add_u32 v46, s43, %[sda]\n"
                    "ds_write2_b64 v46, v[32:33], v[34:35] offset1:32\n"
                    "ds_write2_b64 v46, v[36:37], v[38:39] offset0:128 offset1:160\n"
                    "v_perm_b32 v40, v24, v24, %[e0]\n"
                    "v_perm_b32 v41, v24, v24, %[e1]\n"
                    "v_perm_b32 v42, v24, v24, %[e2]\n"
                    "v_perm_b32 v43, v24, v24, %[e3]\n"
                    "s_waitcnt vmcnt(4)\n"
                    "s_nop 0\n"
                    "v_mfma_f32_16x16x32_bf16 v[112:115], v[40:43], v[208:211], v[112:115]\n"
                    "v_mfma_f32_16x16x32_bf16 v[116:119], v[40:43], v[212:215], v[116:119]\n"
                    "v_mfma_f32_16x16x32_bf16 v[120:123], v[40:43], v[216:219], v[120:123]\n"
                    "v_mfma_f32_16x16x32_bf16 v[124:127], v[40:43], v[220:223], v[124:127]\n"
                    "v_cvt_pk_bf16_f32 v32, v96, v97\n"
                    "v_cvt_pk_bf16_f32 v33, v98, v99\n"
                    "v_cvt_pk_bf16_f32 v34, v100, v101\n"
                    "v_cvt_pk_bf16_f32 v35, v102, v103\n"
                    "v_cvt_pk_bf16_f32 v36, v104, v105\n"
                    "v_cvt_pk_bf16_f32 v37, v106, v107\n"
                    "v_cvt_pk_bf16_f32 v38, v108, v109\n"
                    "v_cvt_pk_bf16_f32 v39, v110, v111\n"
                    "s_cmp_lt_u32 s53, 0x4000\n"
                    "s_cselect_b32 s43, -81920, 0\n"
                    "s_add_u32 s43, s43, s53\n"
                    "v_add_u32 v46, s43, %[sda]\n"
                    "ds_write2_b64 v46, v[32:33], v[34:35] offset1:32\n"
                    "ds_write2_b64 v46, v[36:37], v[38:39] offset0:128 offset1:160\n"
                    "v_perm_b32 v40, v25, v25, %[e0]\n"
                    "v_perm_b32 v41, v25, v25, %[e1]\n"
                    "v_perm_b32 v42, v25, v25, %[e2]\n"
                    "v_perm_b32 v43, v25, v25, %[e3]\n"
                    "s_waitcnt vmcnt(0)\n"
                    "s_nop 0\n"
                    "v_mfma_f32_16x16x32_bf16 v[128:131], v[40:43], v[224:227], v[128:131]\n"
                    "v_mfma_f32_16x16x32_bf16 v[132:135], v[40:43], v[228:231], v[132:135]\n"
                    "v_mfma_f32_16x16x32_bf16 v[136:139], v[40:43], v[232:235], v[136:139]\n"
                    "v_mfma_f32_16x16x32_bf16 v[140:143], v[40:43], v[236:239], v[140:143]\n"
                    "v_cvt_pk_bf16_f32 v32, v112, v113\n"
                    "v_cvt_pk_bf16_f32 v33, v114, v115\n"
                    "v_cvt_pk_bf16_f32 v34, v116, v117\n"
                    "v_cvt_pk_bf16_f32 v35, v118, v119\n"
                    "v_cvt_pk_bf16_f32 v36, v120, v121\n"
                    "v_cvt_pk_bf16_f32 v37, v122, v123\n"
                    "v_cvt_pk_bf16_f32 v38, v124, v125\n"
                    "v_cvt_pk_bf16_f32 v39, v126, v127\n"
                    "s_cmp_lt_u32 s54, 0x4000\n"
                    "s_cselect_b32 s43, -81920, 0\n"
                    "s_add_u32 s43, s43, s54\n"
                    "v_add_u32 v46, s43, %[sda]\n"
                    "ds_write2_b64 v46, v[32:33], v[34:35] offset1:32\n"
                    "ds_write2_b64 v46, v[36:37], v[38:39] offset0:128 offset1:160\n"
                    "s_nop 7\n"
                    "v_cvt_pk_bf16_f32 v32, v128, v129\n"
                    "v_cvt_pk_bf16_f32 v33, v130, v131\n"
                    "v_cvt_pk_bf16_f32 v34, v132, v133\n"
                    "v_cvt_pk_bf16_f32 v35, v134, v135\n"
                    "v_cvt_pk_bf16_f32 v36, v136, v137\n"
                    "v_cvt_pk_bf16_f32 v37, v138, v139\n"
                    "v_cvt_pk_bf16_f32 v38, v140, v141\n"
                    "v_cvt_pk_bf16_f32 v39, v142, v143\n"
                    "s_cmp_lt_u32 s55, 0x4000\n"
                    "s_cselect_b32 s43, -81920, 0\n"
                    "s_add_u32 s43, s43, s55\n"
                    "v_add_u32 v46, s43, %[sda]\n"
                    "ds_write2_b64 v46, v[32:33], v[34:35] offset1:32\n"
                    "ds_write2_b64 v46, v[36:37], v[38:39] offset0:128 offset1:160\n"
                    "s_branch .Lg2_done\n"
                    ".Lg2_spin4:\n"
                    "s_sleep 1\n"
                    "s_sub_u32 s41, s41, 1\n"
                    "s_cbranch_scc1 .Lg2_last4\n"
                    "s_mov_b64 exec, s[46:47]\n"
                    "global_load_dwordx2 v[44:45], %[fo], %[fl] sc1\n"
                    "s_mov_b64 exec, -1\n"
                    ".Lg2_first4:\n"
                    "s_waitcnt vmcnt(0)\n"
                    "v_cmp_eq_u64 s[42:43], %[tag], v[44:45]\n"
                    "s_nop 3\n"
                    "s_and_b32 s42, s42, s40\n"
                    "s_cbranch_scc0 .Lg2_spin4\n"
                    "s_ff1_i32_b32 s44, s42\n"
                    "s_bitset0_b32 s42, s44\n"
                    "s_ff1_i32_b32 s45, s42\n"
                    "s_cmp_lt_i32 s45, 0\n"
                    "s_cbranch_scc0 1f\n"
                    "s_bcnt1_i32_b32 s43, s40\n"
                    "s_cmp_eq_u32 s43, 1\n"
                    "s_cbranch_scc0 .Lg2_spin4\n"
                    "s_mov_b32 s48, 0x80000000\n"
                    "s_mov_b32 s49, 0\n"
                    "s_branch 2f\n"
                    "1:\n"
                    "s_bitset0_b32 s40, s45\n"
                    "s_lshl_b32 s48, s45, 15\n"
                    "s_sub_u32 s49, %[jm], s45\n"
                    "s_lshl_b32 s49, s49, 8\n"
                    "2:\n"
                    "s_bitset0_b32 s40, s44\n"
                    "s_lshl_b32 s43, s44, 15\n"
                    "s_sub_u32 s44, %[jm], s44\n"
                    "s_lshl_b32 s44, s44, 8\n"
                    "v_add_u32 v16, s43, %[lp]\n"
                    "v_add_u32 v17, s44, v18\n"
                    "s_mov_b32 exec_lo, 0\n"
                    "v_add_u32 v16, s48, %[lp]\n"
                    "v_add_u32 v17, s49, v18\n"
                    "s_mov_b32 exec_lo, -1\n"
                    "s_mov_b64 exec, s[46:47]\n"
                    "global_load_dwordx2 v[44:45], %[fo], %[fl] sc1\n"
                    "s_mov_b64 exec, -1\n"
                    "buffer_load_dwordx4 v[144:147], v16, %[srs], s50 offen offset:0 sc1\n"
                    "buffer_load_dwordx4 v[148:151], v16, %[srs], s50 offen offset:256 sc1\n"
                    "buffer_load_dwordx4 v[152:155], v16, %[srs], s50 offen offset:1024 sc1\n"
                    "buffer_load_dwordx4 v[156:159], v16, %[srs], s50 offen offset:1280 sc1\n"
                    "ds_read_u16 v20, v17 offset:0\n"
                    "buffer_load_dwordx4 v[160:163], v16, %[srs], s51 offen offset:0 sc1\n"
                    "buffer_load_dwordx4 v[164:167], v16, %[srs], s51 offen offset:256 sc1\n"
                    "buffer_load_dwordx4 v[168:171], v16, %[srs], s51 offen offset:1024 sc1\n"
                    "buffer_load_dwordx4 v[172:175], v16, %[srs], s51 offen offset:1280 sc1\n"
                    "ds_read_u16 v21, v17 offset:0\n"
                    "buffer_load_dwordx4 v[176:179], v16, %[srs], s52 offen offset:0 sc1\n"
                    "buffer_load_dwordx4 v[180:183], v16, %[srs], s52 offen offset:256 sc1\n"
                    "buffer_load_dwordx4 v[184:187], v16, %[srs], s52 offen offset:1024 sc1\n"
                    "buffer_load_dwordx4 v[188:191], v16, %[srs], s52 offen offset:1280 sc1\n"
                    "ds_read_u16 v22, v17 offset:2\n"
                    "buffer_load_dwordx4 v[192:195], v16, %[srs], s53 offen offset:0 sc1\n"
                    "buffer_load_dwordx4 v[196:199], v16, %[srs], s53 offen offset:256 sc1\n"
                    "buffer_load_dwordx4 v[200:203], v16, %[srs], s53 offen offset:1024 sc1\n"
                    "buffer_load_dwordx4 v[204:207], v16, %[srs], s53 offen offset:1280 sc1\n"
                    "ds_read_u16 v23, v17 offset:2\n"
                    ".Lg2_loop4:\n"
                    "s_cmp_eq_u32 s40, 0\n"
                    "s_cbranch_scc1 .Lg2_last4\n"
                    "s_waitcnt vmcnt(16) lgkmcnt(0)\n"
                    "v_cmp_eq_u64 s[42:43], %[tag], v[44:45]\n"
                    "s_nop 3\n"
                    "s_and_b32 s42, s42, s40\n"
                    "s_cbranch_scc0 .Lg2_drain4\n"
                    "s_ff1_i32_b32 s44, s42\n"
                    "s_bitset0_b32 s42, s44\n"
                    "s_ff1_i32_b32 s45, s42\n"
                    "s_cmp_lt_i32 s45, 0\n"
                    "s_cbranch_scc0 1f\n"
                    "s_bcnt1_i32_b32 s43, s40\n"
                    "s_cmp_eq_u32 s43, 1\n"
                    "s_cbranch_scc0 .Lg2_drain4\n"
                    "s_mov_b32 s48, 0x80000000\n"
                    "s_mov_b32 s49, 0\n"
                    "s_branch 2f\n"
                    "1:\n"
                    "s_bitset0_b32 s40, s45\n"
                    "s_lshl_b32 s48, s45, 15\n"
                    "s_sub_u32 s49, %[jm], s45\n"
                    "s_lshl_b32 s49, s49, 8\n"
                    "2:\n"
                    "s_bitset0_b32 s40, s44\n"
                    "s_lshl_b32 s43, s44, 15\n"
                    "s_sub_u32 s44, %[jm], s44\n"
                    "s_lshl_b32 s44, s44, 8\n"
                    "v_add_u32 v16, s43, %[lp]\n"
                    "v_add_u32 v17, s44, v18\n"
                    "s_mov_b32 exec_lo, 0\n"
                    "v_add_u32 v16, s48, %[lp]\n"
                    "v_add_u32 v17, s49, v18\n"
                    "s_mov_b32 exec_lo, -1\n"
                    "s_mov_b64 exec, s[46:47]\n"
                    "global_load_dwordx2 v[44:45], %[fo], %[fl] sc1\n"
                    "s_mov_b64 exec, -1\n"
                    "v_perm_b32 v40, v20, v20, %[e0]\n"
                    "v_perm_b32 v41, v20, v20, %[e1]\n"
                    "v_perm_b32 v42, v20, v20, %[e2]\n"
                    "v_perm_b32 v43, v20, v20, %[e3]\n"
                    "s_waitcnt vmcnt(13)\n"
                    "s_nop 0\n"
                    "v_mfma_f32_16x16x32_bf16 v[48:51], v[40:43], v[144:147], v[48:51]\n"
                    "v_mfma_f32_16x16x32_bf16 v[52:55], v[40:43], v[148:151], v[52:55]\n"
                    "buffer_load_dwordx4 v[144:147], v16, %[srs], s50 offen offset:0 sc1\n"
                    "v_mfma_f32_16x16x32_bf16 v[56:59], v[40:43], v[152:155], v[56:59]\n"
                    "buffer_load_dwordx4 v[148:151], v16, %[srs], s50 offen offset:256 sc1\n"
                    "v_mfma_f32_16x16x32_bf16 v[60:63], v[40:43], v[156:159], v[60:63]\n"
                    "buffer_load_dwordx4 v[152:155], v16, %[srs], s50 offen offset:1024 sc1\n"
                    "buffer_load_dwordx4 v[156:159], v16, %[srs], s50 offen offset:1280 sc1\n"
                    "ds_read_u16 v20, v17 offset:0\n"
                    "v_perm_b32 v40, v21, v21, %[e0]\n"
                    "v_perm_b32 v41, v21, v21, %[e1]\n"
                    "v_perm_b32 v42, v21, v21, %[e2]\n"
                    "v_perm_b32 v43, v21, v21, %[e3]\n"
                    "s_waitcnt vmcnt(13)\n"
                    "s_nop 0\n"
                    "v_mfma_f32_16x16x32_bf16 v[64:67], v[40:43], v[160:163], v[64:67]\n"
                    "v_mfma_f32_16x16x32_bf16 v[68:71], v[40:43], v[164:167], v[68:71]\n"
                    "buffer_load_dwordx4 v[160:163], v16, %[srs], s51 offen offset:0 sc1\n"
                    "v_mfma_f32_16x16x32_bf16 v[72:75], v[40:43], v[168:171], v[72:75]\n"
                    "buffer_load_dwordx4 v[164:167], v16, %[srs], s51 offen offset:256 sc1\n"
                    "v_mfma_f32_16x16x32_bf16 v[76:79], v[40:43], v[172:175], v[76:79]\n"
                    "buffer_load_dwordx4 v[168:171], v16, %[srs], s51 offen offset:1024 sc1\n"
                    "buffer_load_dwordx4 v[172:175], v16, %[srs], s51 offen offset:1280 sc1\n"
                    "ds_read_u16 v21, v17 offset:0\n"
                    "v_perm_b32 v40, v22, v22, %[e0]\n"
                    "v_perm_b32 v41, v22, v22, %[e1]\n"
                    "v_perm_b32 v42, v22, v22, %[e2]\n"
                    "v_perm_b32 v43, v22, v22, %[e3]\n"
                    "s_waitcnt vmcnt(13)\n"
                    "s_nop 0\n"
                    "v_mfma_f32_16x16x32_bf16 v[80:83], v[40:43], v[176:179], v[80:83]\n"
                    "v_mfma_f32_16x16x32_bf16 v[84:87], v[40:43], v[180:183], v[84:87]\n"
                    "buffer_load_dwordx4 v[176:179], v16, %[srs], s52 offen offset:0 sc1\n"
                    "v_mfma_f32_16x16x32_bf16 v[88:91], v[40:43], v[184:187], v[88:91]\n"
                    "buffer_load_dwordx4 v[180:183], v16, %[srs], s52 offen offset:256 sc1\n"
                    "v_mfma_f32_16x16x32_bf16 v[92:95], v[40:43], v[188:191], v[92:95]\n"
                    "buffer_load_dwordx4 v[184:187], v16, %[srs], s52 offen offset:1024 sc1\n"
                    "buffer_load_dwordx4 v[188:191], v16, %[srs], s52 offen offset:1280 sc1\n"
                    "ds_read_u16 v22, v17 offset:2\n"
                    "v_perm_b32 v40, v23, v23, %[e0]\n"
                    "v_perm_b32 v41, v23, v23, %[e1]\n"
                    "v_perm_b32 v42, v23, v23, %[e2]\n"
                    "v_perm_b32 v43, v23, v23, %[e3]\n"
                    "s_waitcnt vmcnt(13)\n"
                    "s_nop 0\n"
                    "v_mfma_f32_16x16x32_bf16 v[96:99], v[40:43], v[192:195], v[96:99]\n"
                    "v_mfma_f32_16x16x32_bf16 v[100:103], v[40:43], v[196:199], v[100:103]\n"
                    "buffer_load_dwordx4 v[192:195], v16, %[srs], s53 offen offset:0 sc1\n"
                    "v_mfma_f32_16x16x32_bf16 v[104:107], v[40:43], v[200:203], v[104:107]\n"
                    "buffer_load_dwordx4 v[196:199], v16, %[srs], s53 offen offset:256 sc1\n"
                    "v_mfma_f32_16x16x32_bf16 v[108:111], v[40:43], v[204:207], v[108:111]\n"
                    "buffer_load_dwordx4 v[200:203], v16, %[srs], s53 offen offset:1024 sc1\n"
                    "buffer_load_dwordx4 v[204:207], v16, %[srs], s53 offen offset:1280 sc1\n"
                    "ds_read_u16 v23, v17 offset:2\n"
                    "s_branch .Lg2_loop4\n"
                    ".Lg2_drain4:\n"
                    "s_mov_b64 exec, s[46:47]\n"
                    "global_load_dwordx2 v[44:45], %[fo], %[fl] sc1\n"
                    "s_mov_b64 exec, -1\n"
                    "v_perm_b32 v40, v20, v20, %[e0]\n"
                    "v_perm_b32 v41, v20, v20, %[e1]\n"
                    "v_perm_b32 v42, v20, v20, %[e2]\n"
                    "v_perm_b32 v43, v20, v20, %[e3]\n"
                    "s_waitcnt vmcnt(13)\n"
                    "s_nop 0\n"
                    "v_mfma_f32_16x16x32_bf16 v[48:51], v[40:43], v[144:147], v[48:51]\n"
                    "v_mfma_f32_16x16x32_bf16 v[52:55], v[40:43], v[148:151], v[52:55]\n"
                    "v_mfma_f32_16x16x32_bf16 v[56:59], v[40:43], v[152:155], v[56:59]\n"
                    "v_mfma_f32_16x16x32_bf16 v[60:63], v[40:43], v[156:159], v[60:63]\n"
                    "v_perm_b32 v40, v21, v21, %[e0]\n"
                    "v_perm_b32 v41, v21, v21, %[e1]\n"
                    "v_perm_b32 v42, v21, v21, %[e2]\n"
                    "v_perm_b32 v43, v21, v21, %[e3]\n"
                    "s_waitcnt vmcnt(9)\n"
                    "s_nop 0\n"
                    "v_mfma_f32_16x16x32_bf16 v[64:67], v[40:43], v[160:163], v[64:67]\n"
                    "v_mfma_f32_16x16x32_bf16 v[68:71], v[40:43], v[164:167], v[68:71]\n"
                    "v_mfma_f32_16x16x32_bf16 v[72:75], v[40:43], v[168:171], v[72:75]\n"
                    "v_mfma_f32_16x16x32_bf16 v[76:79], v[40:43], v[172:175], v[76:79]\n"
                    "v_perm_b32 v40, v22, v22, %[e0]\n"
                    "v_perm_b32 v41, v22, v22, %[e1]\n"
                    "v_perm_b32 v42, v22, v22, %[e2]\n"
                    "v_perm_b32 v43, v22, v22, %[e3]\n"
                    "s_waitcnt vmcnt(5)\n"
                    "s_nop 0\n"
                    "v_mfma_f32_16x16x32_bf16 v[80:83], v[40:43], v[176:179], v[80:83]\n"
                    "v_mfma_f32_16x16x32_bf16 v[84:87], v[40:43], v[180:183], v[84:87]\n"
                    "v_mfma_f32_16x16x32_bf16 v[88:91], v[40:43], v[184:187], v[88:91]\n"
                    "v_mfma_f32_16x16x32_bf16 v[92:95], v[40:43], v[188:191], v[92:95]\n"
                    "v_perm_b32 v40, v23, v23, %[e0]\n"
                    "v_perm_b32 v41, v23, v23, %[e1]\n"
                    "v_perm_b32 v42, v23, v23, %[e2]\n"
                    "v_perm_b32 v43, v23, v23, %[e3]\n"
                    "s_waitcnt vmcnt(1)\n"
                    "s_nop 0\n"
                    "v_mfma_f32_16x16x32_bf16 v[96:99], v[40:43], v[192:195], v[96:99]\n"
                    "v_mfma_f32_16x16x32_bf16 v[100:103], v[40:43], v[196:199], v[100:103]\n"
                    "v_mfma_f32_16x16x32_bf16 v[104:107], v[40:43], v[200:203], v[104:107]\n"
                    "v_mfma_f32_16x16x32_bf16 v[108:111], v[40:43], v[204:207], v[108:111]\n"
                    "s_branch .Lg2_first4\n"
                    ".Lg2_last4:\n"
                    "v_mov_b32 v46, %[vfa]\n"
                    "s_mov_b32 s43, 0x400000\n"
                    "3:\n"
                    "ds_read_b32 v47, v46\n"
                    "s_waitcnt lgkmcnt(0)\n"
                    "v_readfirstlane_b32 s44, v47\n"
                    "s_nop 0\n"
                    "s_cmp_ge_u32 s44, 12\n"
                    "s_cbranch_scc1 4f\n"
                    "s_sleep 1\n"
                    "s_sub_u32 s43, s43, 1\n"
                    "s_cbranch_scc0 3b\n"
                    "4:\n"
                    "v_perm_b32 v40, v20, v20, %[e0]\n"
                    "v_perm_b32 v41, v20, v20, %[e1]\n"
                    "v_perm_b32 v42, v20, v20, %[e2]\n"
                    "v_perm_b32 v43, v20, v20, %[e3]\n"
                    "s_waitcnt vmcnt(12)\n"
                    "s_nop 0\n"
                    "v_mfma_f32_16x16x32_bf16 v[48:51], v[40:43], v[144:147], v[48:51]\n"
                    "v_mfma_f32_16x16x32_bf16 v[52:55], v[40:43], v[148:151], v[52:55]\n"
                    "v_mfma_f32_16x16x32_bf16 v[56:59], v[40:43], v[152:155], v[56:59]\n"
                    "v_mfma_f32_16x16x32_bf16 v[60:63], v[40:43], v[156:159], v[60:63]\n"
                    "v_perm_b32 v40, v21, v21, %[e0]\n"
                    "v_perm_b32 v41, v21, v21, %[e1]\n"
                    "v_perm_b32 v42, v21, v21, %[e2]\n"
                    "v_perm_b32 v43, v21, v21, %[e3]\n"
                    "s_waitcnt vmcnt(8)\n"
                    "s_nop 0\n"
                    "v_mfma_f32_16x16x32_bf16 v[64:67], v[40:43], v[160:163], v[64:67]\n"
                    "v_mfma_f32_16x16x32_bf16 v[68:71], v[40:43], v[164:167], v[68:71]\n"
                    "v_mfma_f32_16x16x32_bf16 v[72:75], v[40:43], v[168:171], v[72:75]\n"
                    "v_mfma_f32_16x16x32_bf16 v[76:79], v[40:43], v[172:175], v[76:79]\n"
                    "v_cvt_pk_bf16_f32 v32, v48, v49\n"
                    "v_cvt_pk_bf16_f32 v33, v50, v51\n"
                    "v_cvt_pk_bf16_f32 v34, v52, v53\n"
                    "v_cvt_pk_bf16_f32 v35, v54, v55\n"
                    "v_cvt_pk_bf16_f32 v36, v56, v57\n"
                    "v_cvt_pk_bf16_f32 v37, v58, v59\n"
                    "v_cvt_pk_bf16_f32 v38, v60, v61\n"
                    "v_cvt_pk_bf16_f32 v39, v62, v63\n"
                    "s_cmp_lt_u32 s50, 0x4000\n"
                    "s_cselect_b32 s43, -81920, 0\n"
                    "s_add_u32 s43, s43, s50\n"
                    "v_add_u32 v46, s43, %[sda]\n"
                    "ds_write2_b64 v46, v[32:33], v[34:35] offset1:32\n"
                    "ds_write2_b64 v46, v[36:37], v[38:39] offset0:128 offset1:160\n"
                    "v_perm_b32 v40, v22, v22, %[e0]\n"
                    "v_perm_b32 v41, v22, v22, %[e1]\n"
                    "v_perm_b32 v42, v22, v22, %[e2]\n"
                    "v_perm_b32 v43, v22, v22, %[e3]\n"
                    "s_waitcnt vmcnt(4)\n"
                    "s_nop 0\n"
                    "v_mfma_f32_16x16x32_bf16 v[80:83], v[40:43], v[176:179], v[80:83]\n"
                    "v_mfma_f32_16x16x32_bf16 v[84:87], v[40:43], v[180:183], v[84:87]\n"
                    "v_mfma_f32_16x16x32_bf16 v[88:91], v[40:43], v[184:187], v[88:91]\n"
                    "v_mfma_f32_16x16x32_bf16 v[92:95], v[40:43], v[188:191], v[92:95]\n"
                    "v_cvt_pk_bf16_f32 v32, v64, v65\n"
                    "v_cvt_pk_bf16_f32 v33, v66, v67\n"
                    "v_cvt_pk_bf16_f32 v34, v68, v69\n"
                    "v_cvt_pk_bf16_f32 v35, v70, v71\n"
                    "v_cvt_pk_bf16_f32 v36, v72, v73\n"
                    "v_cvt_pk_bf16_f32 v37, v74, v75\n"
                    "v_cvt_pk_bf16_f32 v38, v76, v77\n"
                    "v_cvt_pk_bf16_f32 v39, v78, v79\n"
                    "s_cmp_lt_u32 s51, 0x4000\n"
                    "s_cselect_b32 s43, -81920, 0\n"
                    "s_add_u32 s43, s43, s51\n"
                    "v_add_u32 v46, s43, %[sda]\n"
                    "ds_write2_b64 v46, v[32:33], v[34:35] offset1:32\n"
                    "ds_write2_b64 v46, v[36:37], v[38:39] offset0:128 offset1:160\n"
                    "v_perm_b32 v40, v23, v23, %[e0]\n"
                    "v_perm_b32 v41, v23, v23, %[e1]\n"
                    "v_perm_b32 v42, v23, v23, %[e2]\n"
                    "v_perm_b32 v43, v23, v23, %[e3]\n"
                    "s_waitcnt vmcnt(0)\n"
                    "s_nop 0\n"
                    "v_mfma_f32_16x16x32_bf16 v[96:99], v[40:43], v[192:195], v[96:99]\n"
                    "v_mfma_f32_16x16x32_bf16 v[100:103], v[40:43], v[196:199], v[100:103]\n"
                    "v_mfma_f32_16x16x32_bf16 v[104:107], v[40:43], v[200:203], v[104:107]\n"
                    "v_mfma_f32_16x16x32_bf16 v[108:111], v[40:43], v[204:207], v[108:111]\n"
                    "v_cvt_pk_bf16_f32 v32, v80, v81\n"
                    "v_cvt_pk_bf16_f32 v33, v82, v83\n"
                    "v_cvt_pk_bf16_f32 v34, v84, v85\n"
                    "v_cvt_pk_bf16_f32 v35, v86, v87\n"
                    "v_cvt_pk_bf16_f32 v36, v88, v89\n"
                    "v_cvt_pk_bf16_f32 v37, v90, v91\n"
                    "v_cvt_pk_bf16_f32 v38, v92, v93\n"
                    "v_cvt_pk_bf16_f32 v39, v94, v95\n"
                    "s_cmp_lt_u32 s52, 0x4000\n"
                    "s_cselect_b32 s43, -81920, 0\n"
                    "s_add_u32 s43, s43, s52\n"
                    "v_add_u32 v46, s43, %[sda]\n"
                    "ds_write2_b64 v46, v[32:33], v[34:35] offset1:32\n"
                    "ds_write2_b64 v46, v[36:37], v[38:39] offset0:128 offset1:160\n"
                    "s_nop 7\n"
                    "v_cvt_pk_bf16_f32 v32, v96, v97\n"
                    "v_cvt_pk_bf16_f32 v33, v98, v99\n"
                    "v_cvt_pk_bf16_f32 v34, v100, v101\n"
                    "v_cvt_pk_bf16_f32 v35, v102, v103\n"
                    "v_cvt_pk_bf16_f32 v36, v104, v105\n"
                    "v_cvt_pk_bf16_f32 v37, v106, v107\n"
                    "v_cvt_pk_bf16_f32 v38, v108, v109\n"
                    "v_cvt_pk_bf16_f32 v39, v110, v111\n"
                    "s_cmp_lt_u32 s53, 0x4000\n"
                    "s_cselect_b32 s43, -81920, 0\n"
                    "s_add_u32 s43, s43, s53\n"
                    "v_add_u32 v46, s43, %[sda]\n"
                    "ds_write2_b64 v46, v[32:33], v[34:35] offset1:32\n"
                    "ds_write2_b64 v46, v[36:37], v[38:39] offset0:128 offset1:160\n"
                    ".Lg2_done:\n"
                    "s_waitcnt vmcnt(0)\n"
                    :: [lp] "v"(lp), [wl] "v"(smb + L_WB + r16 * 16), [sda] "v"(smb + L_V + 32768 + lp + (g4 >> 1) * 8), [fo] "v"(lane * 128),
                       [e0] "v"(sel[0]), [e1] "v"(sel[1]), [e2] "v"(sel[2]), [e3] "v"(sel[3]),
                       [srs] "s"(srs), [fl] "s"(flags), [tag] "s"(tag), [full] "s"((1u << j) - 1), [jm] "s"(j - 1), [g0] "s"(c * 6 * 2048), [vfa] "s"(fb + 4 * F_VF)
                    : "memory", "scc", "v16", "v17", "v18", "v19", "v20", "v21", "v22", "v23", "v24", "v25", "v26", "v27", "v28", "v29", "v30", "v31", "v32", "v33", "v34", "v35", "v36", "v37", "v38", "v39", "v40", "v41", "v42", "v43", "v44", "v45", "v46", "v47", "v48", "v49", "v50", "v51", "v52", "v53", "v54", "v55", "v56", "v57", "v58", "v59", "v60", "v61", "v62", "v63", "v64", "v65", "v66", "v67", "v68", "v69", "v70", "v71", "v72", "v73", "v74", "v75", "v76", "v77", "v78", "v79", "v80", "v81", "v82", "v83", "v84", "v85", "v86", "v87", "v88", "v89", "v90", "v91", "v92", "v93", "v94", "v95", "v96", "v97", "v98", "v99", "v100", "v101", "v102", "v103", "v104", "v105", "v106", "v107", "v108", "v109", "v110", "v111", "v112", "v113", "v114", "v115", "v116", "v117", "v118", "v119", "v120", "v121", "v122", "v123", "v124", "v125", "v126", "v127", "v128", "v129", "v130", "v131", "v132", "v133", "v134", "v135", "v136", "v137", "v138", "v139", "v140", "v141", "v142", "v143", "v144", "v145", "v146", "v147", "v148", "v149", "v150", "v151", "v152", "v153", "v154", "v155", "v156", "v157", "v158", "v159", "v160", "v161", "v162", "v163", "v164", "v165", "v166", "v167", "v168", "v169", "v170", "v171", "v172", "v173", "v174", "v175", "v176", "v177", "v178", "v179", "v180", "v181", "v182", "v183", "v184", "v185", "v186", "v187", "v188", "v189", "v190", "v191", "v192", "v193", "v194", "v195", "v196", "v197", "v198", "v199", "v200", "v201", "v202", "v203", "v204", "v205", "v206", "v207", "v208", "v209", "v210", "v211", "v212", "v213", "v214", "v215", "v216", "v217", "v218", "v219", "v220", "v221", "v222", "v223", "v224", "v225", "v226", "v227", "v228", "v229", "v230", "v231", "v232", "v233", "v234", "v235", "v236", "v237", "v238", "v239", "s40", "s41", "s42", "s43", "s44", "s45", "s46", "s47", "s48", "s49", "s50", "s51", "s52", "s53", "s54", "s55");
                sig(fb, F_SR);
                if (j == 7 && c == 0 && lane_id() == 0) __hip_atomic_store(flags + 128, tag, RLX_AGENT);
            }
        }
        {
            __builtin_amdgcn_s_setprio(0);
            const __amdgpu_buffer_rsrc_t ors = __builtin_amdgcn_make_buffer_rsrc((void*)((char*)out_ + segrow * 256), 0, 65536, 0x00020000);
            const u32 lane = lane_id();
            const u32 rr = lane >> 4, ch = lane & 15;
            u32 l16 = lane * 16;
            LAS char* rb0 = sm + (16 * c + rr) * 256 + ((ch ^ rr) << 4);
            LAS char* rb1 = sm + (16 * c + rr) * 256 + ((ch ^ rr ^ 4) << 4);
            UNROLL for (int cc = 0; cc < 4; ++cc) {
                const u32 slot = OSLOT(cc);
                u32x4 x[4];
                wait_ge(fb, F_OC + cc, 4);
                x[0] = *(LAS u32x4*)(rb0 + slot); x[1] = *(LAS u32x4*)(rb1 + slot + 1024);
                x[2] = *(LAS u32x4*)(rb0 + slot + 2048); x[3] = *(LAS u32x4*)(rb1 + slot + 3072);
                PIN(l16);
                UNROLL for (int i = 0; i < 4; ++i)
                    __builtin_amdgcn_raw_buffer_store_b128(x[i], ors, l16 + i * 1024, cc * 16384 + c * 4096, 16);
            }
        }
        return;
        }
    } else {
        const int w = wave - 4;
        u32 r = lane & 31, h = lane >> 5, blk = (lane >> 4) & 1, qq = (lane & 15) >> 2, pp = lane & 3;
        wait_ge(fb, F_GI, 2);
#define NTS " nt"
#define DMA4(voff, src, dst) asm volatile("s_mov_b32 m0, %2\n\ts_nop 0\n\t" \
                             "global_load_lds_dwordx4 %0, %1" NTS "\n\t" \
                             "global_load_lds_dwordx4 %0, %1 offset:1024" NTS "\n\t" \
                             "global_load_lds_dwordx4 %0, %1 offset:2048" NTS "\n\t" \
                             "global_load_lds_dwordx4 %0, %1 offset:3072" NTS \
                             :: "v"(voff), "s"(src), "s"(dst) : "memory")
        const u32 koff = lane * 16;
        u32 voff = (lane >> 4) * 256 + (((lane & 15) ^ ((lane >> 4) << 2)) << 4);
        const char* kb = (const char*)k_ + (segrow + w * 16) * 256;
        const char* vb = (const char*)v_ + (segrow + w * 16) * 256;
        const u32 ldsbase = (u32)(uintptr_t)(sm + w * 4096);
#define UNIT(u) do { if ((u) < 4) DMA4(koff, kb + (u) * 16384, ldsbase + L_KT + (u) * 16384); else if ((u) < 8) DMA4(voff, vb + ((u) - 4) * 16384, ldsbase + L_V + ((u) - 4) * 16384); } while (0)
#define WAITU(u) do { constexpr int n_ = 4 * (((u) + WIN - 1 < 7 ? (u) + WIN - 1 : 7) - (u)); asm volatile("s_waitcnt vmcnt(%0)" :: "i"(n_) : "memory"); } while (0)
        UNROLL for (int u = 0; u < WIN; ++u) UNIT(u);
        WAITU(0); sig(fb, F_KL + 0); UNIT(WIN);
        WAITU(1); sig(fb, F_KL + 1); UNIT(WIN + 1);
        WAITU(2); sig(fb, F_KL + 2); UNIT(WIN + 2);
        WAITU(3); sig(fb, F_KL + 3); UNIT(WIN + 3);
        __builtin_amdgcn_s_setprio(3);
        const u32 fr = ((r >> 2) & 7) ^ ((r & 2) << 1);
        LAS char* krow[4];
        UNROLL for (int ks = 0; ks < 4; ++ks) { u32 a_ = L_KT + r * 128 + (((2 * ks + h) ^ fr) << 4); PIN(a_); krow[ks] = sm + a_; }
        const u32 eq = 4 * blk + (((pp & 1) << 1) | (pp >> 1));
        u32 vna = L_V + (8 * h + qq) * 256 + ((4 * (w ^ qq) + (eq >> 1)) << 4) + 8 * (eq & 1);
        PIN(vna);
        LAS char* vnat = sm + vna;
        u32 l16 = lane * 16;
        PIN(l16);
        LAS char* decr = sm + L_DEC + (h * 16 + (lane & 15)) * 16;
        f32x16 W[4];
        bf16x8 Sf[4][8];
        __amdgpu_buffer_rsrc_t prs;
        bool bad = false;
        u32x4 badv;
        wait_ge(fb, F_KR, 1);
        bf16x8 Kf[4][4];
        UNROLL for (int mt = 0; mt < 2; ++mt)
            UNROLL for (int ks = 0; ks < 4; ++ks) Kf[mt][ks] = *(LAS bf16x8*)(krow[ks] + mt * 4096);
        f32x4 dv[4];
        u32x2 pkl;
#define SCALEQ(cs, m1, ks) do { \
            if (ks == 0) scale4<0>(W[m1], dv[cs][m1]); \
            if (ks == 1) scale4<1>(W[m1], dv[cs][m1]); \
            if (ks == 2) scale4<2>(W[m1], dv[cs][m1]); \
            if (ks == 3) scale4<3>(W[m1], dv[cs][m1]); } while (0)
#define PACKQ(cs, m1, ks) do { \
            if (!(ks & 1)) { pkl = (u32x2){pk(W[m1][4 * ks], W[m1][4 * ks + 1]), pk(W[m1][4 * ks + 2], W[m1][4 * ks + 3])}; PIN(pkl); } \
            else { \
                const u32x4 fr_ = {pkl[0], pkl[1], pk(W[m1][4 * ks], W[m1][4 * ks + 1]), pk(W[m1][4 * ks + 2], W[m1][4 * ks + 3])}; \
                if (cs < 3) { Sf[(cs + 1) & 3][2 * (m1) + (ks >> 1)] = __builtin_bit_cast(bf16x8, fr_); PIN(Sf[(cs + 1) & 3][2 * (m1) + (ks >> 1)]); } \
                else __builtin_amdgcn_raw_buffer_store_b128(fr_, prs, l16, (2 * (m1) + (ks >> 1)) * 4096, 16); \
            } } while (0)
#define VFRAG(c, ks) trfrag(vnat + (c) * 16384 + (ks) * 4096, vnat + (c) * 16384 + (ks) * 4096 + 1024)
        bf16x8 Vf[4][4];
        u32 vfl = 0, krn = 1;
        dv[0] = *(LAS f32x4*)(decr);
        WAITU(4);
        sig(fb, F_VL);
        UNIT(WIN + 4);
        static_for<0, 4>([&](auto cc_) __attribute__((always_inline)) {
            constexpr int c = decltype(cc_)::value;
            if (c == 0 || NOT_YET(vfl, 4)) {
                SLOW_WAIT(F_VL + c, 4);
                UNROLL for (int ks = 0; ks < 4; ++ks) Vf[c][ks] = VFRAG(c, ks);
            }
            MEMFENCE();
            static_for<0, 16>([&](auto tt_) __attribute__((always_inline)) {
                constexpr int t = decltype(tt_)::value, mt = t >> 2, ks = t & 3;
                if constexpr (t == 8 && c < 3) { if (NOT_YET(krn, 1)) SLOW_WAIT(F_KR + c + 1, 1); MEMFENCE(); }
                if constexpr (c == 0 && ks == 0) { f32x16 z; UNROLL for (int i = 0; i < 16; ++i) z[i] = 0.f; W[mt] = MFMA(Kf[mt][ks], Vf[c][ks], z); }
                else W[mt] = MFMA(Kf[mt][ks], Vf[c][ks], W[mt]);
                SCHED();
                if constexpr (mt < 2) Kf[mt + 2][ks] = *(LAS bf16x8*)(krow[ks] + c * 16384 + (mt + 2) * 4096);
                else if constexpr (c < 3) Kf[mt - 2][ks] = *(LAS bf16x8*)(krow[ks] + (c + 1) * 16384 + (mt - 2) * 4096);
                if constexpr (c < 3) {
                    if constexpr (t == 3) { MEMFENCE(); krn = FLAG(F_KR + c + 1); MEMFENCE(); }
                    if constexpr (t == 9) {
                        dv[c + 1] = *(LAS f32x4*)(decr + (c + 1) * 512);
                        WAITU(5 + c);
                        sig(fb, F_VL + c + 1);
                        UNIT(WIN + 5 + c);
                    }
                    if constexpr (c == 2 && t == 10) badv = *(volatile LAS u32x4*)(sm + L_FLG + F_BAD * 4);
                    if constexpr (c == 2 && t == 15) {
                        bad = __builtin_amdgcn_readfirstlane(badv[0] | badv[1] | badv[2] | badv[3]) != 0;
                        prs = __builtin_amdgcn_make_buffer_rsrc((void*)(ws_ + WS_S + (size_t)(head * 8 + j) * 32768 + w * 1024), 0, j < 7 && !bad ? 29696 : 0, 0x00020000);
                    }
                    if constexpr (t == 12) { MEMFENCE(); vfl = FLAG(F_VL + c + 1); MEMFENCE(); }
                    if constexpr (t >= 12) Vf[c + 1][ks] = VFRAG(c + 1, ks);
                }
                constexpr int sp = t - 6, ss = t - 5;
                if constexpr (sp >= 0) PACKQ(c, (sp >> 2), (sp & 3));
                else if constexpr (c > 0) PACKQ(c - 1, ((sp + 16) >> 2), ((sp + 16) & 3));
                if constexpr (ss >= 0) SCALEQ(c, (ss >> 2), (ss & 3));
                else if constexpr (c > 0) SCALEQ(c - 1, ((ss + 16) >> 2), ((ss + 16) & 3));
                SCHED();
            });
            MEMFENCE();
        });
        PACKQ(3, 2, 2);
        UNROLL for (int st = 11; st < 16; ++st) { SCALEQ(3, (st >> 2), (st & 3)); if (st > 11) PACKQ(3, ((st - 1) >> 2), ((st - 1) & 3)); }
        PACKQ(3, 3, 3);
        if (__builtin_expect(bad, 0)) goto slow_path;
        {
        PIN(l16);
        r = (l16 >> 4) & 31; h = l16 >> 9; blk = (l16 >> 8) & 1; qq = (l16 >> 6) & 3; pp = (l16 >> 4) & 3;
        u32 vpa = vna - h * 1024;
        PIN(vpa);
        LAS char* vper = sm + vpa;
        u32 tro[2];
        UNROLL for (int jj = 0; jj < 2; ++jj)
            tro[jj] = (8 * jj + 4 * h + qq) * 128 + (((4 * (qq >> 1)) + 2 * (blk ^ jj) + ((pp >> 1) ^ h)) << 4) + 8 * (pp & 1);
        bf16x8 Vp[4][4];
#define VPRE(c) do { UNROLL for (int ks = 0; ks < 4; ++ks) Vp[c][ks] = trfrag(vper + (c) * 16384 + ks * 4096, vper + (c) * 16384 + ks * 4096 + 2048); if ((c) > 0) sig(fb, F_VF); } while (0)
        VPRE(0);
        VPRE(1);
        f32x16 o[4][2];
#define TRB(ta, base, x) UNROLL for (int pa = 0; pa < 2; ++pa) UNROLL for (int jj = 0; jj < 2; ++jj) { u32 a_ = (base) + (tro[jj] ^ (x) ^ (pa << 6)); PIN(a_); ta[pa][jj] = sm + a_; }
#define TRF(ta, off, kk, tl) trfrag(ta[((kk) ^ (tl)) & 1][0] + (off) + (kk) * 2048, ta[((kk) ^ (tl)) & 1][1] + (off) + (kk) * 2048)
        LAS char* tq[2][2];
        TRB(tq, L_V, 0)
        u32 atr = L_AT + l16;
        PIN(atr);
        {
            LAS char* tk[2][2]; LAS char* tx[2][2];
            u32 atw;
            u32x4 msk[2];
            {
                const u32 sg = w == 3 ? 64 : 0, tu = w == 1 ? 0 : 64;
                TRB(tk, L_KT, sg) TRB(tx, L_X0, tu)
                atw = L_AT + (w == 0 ? 1 : w - 1) * 2048 + l16;
                PIN(atw);
                const int thr = ((w & 1) ? (int)r : 64) - 4 * (int)h;
                UNROLL for (int i = 0; i < 8; ++i) {
                    const int s0 = ((2 * i) & 3) + 8 * (i >> 1);
                    msk[i >> 2][i & 3] = (s0 <= thr ? 0xffffu : 0u) | (s0 + 1 <= thr ? 0xffff0000u : 0u);
                }
                PIN(msk[0]); PIN(msk[1]);
            }
            bf16x8 Ka[4][8], Qa[4][8], Qs[4][16];
            u32 qfl;
#define QFIRST(c) do { UNROLL for (int kk = 0; kk < 3; ++kk) Qa[c][kk] = TRF(tx, (c) == 2 ? 16384 : 0, kk, 0); } while (0)
#define XPRE(c) do { UNROLL for (int kk = 0; kk < 3; ++kk) Ka[c][kk] = TRF(tk, (c) * 16384, kk, 0); \
                MEMFENCE(); qfl = FLAG(F_QR + (c)); MEMFENCE(); QFIRST(c); MEMFENCE(); } while (0)
            bool pub_done = j == 7;
            u64 tag;
            { const u64 tv = *(LAS u64*)(sm + L_TAG); tag = ((u64)(u32)__builtin_amdgcn_readfirstlane((u32)(tv >> 32)) << 32) | (u32)__builtin_amdgcn_readfirstlane((u32)tv); }
#define PUBLISH() do { if (!pub_done) { pub_done = true; \
                asm volatile("s_waitcnt vmcnt(0)" ::: "memory"); \
                if (atr == L_AT) { \
                    u32 old = __hip_atomic_fetch_add((LAS u32*)(sm + L_FLG) + F_PUB, 1u, RLX_WG); \
                    if (old == 3) __hip_atomic_store(flags + j * 16, tag, RLX_AGENT); \
                } \
                } } while (0)
            XPRE(0);
            UNROLL for (int c = 0; c < 4; ++c) {
                const u32 qo = c == 2 ? 16384 : 0;
                const u32 ao = (c & 1) * 6144;
                if (NOT_YET(qfl, 1)) { if (c == 0) PUBLISH(); SLOW_WAIT(F_QR + c, 1); QFIRST(c); }
                MEMFENCE();
                f32x16 X;
                {
                    UNROLL for (int kk = 0; kk < 8; ++kk) {
                        if (kk == 0) { f32x16 z; UNROLL for (int i = 0; i < 16; ++i) z[i] = 0.f; X = MFMA(Ka[c][kk], Qa[c][kk], z); }
                        else X = MFMA(Ka[c][kk], Qa[c][kk], X);
                        SCHED();
                        if (kk + 3 < 8) { Ka[c][kk + 3] = TRF(tk, c * 16384, kk + 3, 0); Qa[c][kk + 3] = TRF(tx, qo, kk + 3, 0); }
                        else if (c > 0 && kk < 7) { Qs[c][2 * kk - 10] = TRF(tq, qo, 2 * kk - 10, 0); Qs[c][2 * kk - 9] = TRF(tq, qo, 2 * kk - 9, 0); }
                        SCHED();
                    }
                    if (c == 0) { UNROLL for (int pa = 0; pa < 2; ++pa) UNROLL for (int jj = 0; jj < 2; ++jj) { u32 a_ = (u32)(tx[pa][jj] - sm) + L_V - L_X0; PIN(a_); tx[pa][jj] = sm + a_; } }
                }
                bf16x8 At[6];
                u32 afl = 0;
                if (c > 0) {
                    u32x4 xp[2];
                    UNROLL for (int s = 0; s < 16; ++s) {
                        if (ord_kk(s) == 0) { f32x16 z; UNROLL for (int i = 0; i < 16; ++i) z[i] = 0.f; o[c][ord_tl(s)] = MFMA(Sf[c][ord_kk(s)], Qs[c][s], z); }
                        else o[c][ord_tl(s)] = MFMA(Sf[c][ord_kk(s)], Qs[c][s], o[c][ord_tl(s)]);
                        SCHED();
                        if (s + 4 < 16) Qs[c][s + 4] = TRF(tq, qo, ord_kk(s + 4), ord_tl(s + 4));
                        {
                            if (s >= 1 && s < 5) { UNROLL for (int i = 2 * s - 2; i < 2 * s; ++i) xp[i >> 2][i & 3] = pk(X[2 * i], X[2 * i + 1]) & msk[i >> 2][i & 3]; }
                            if (s == 3) *(LAS u32x4*)(sm + atw + ao) = xp[0];
                            if (s == 5) *(LAS u32x4*)(sm + atw + ao + 1024) = xp[1];
                            if (s == 6) sig(fb, F_AT + c);
                            if (s == 12) { MEMFENCE(); afl = FLAG(F_AT + c); MEMFENCE(); }
                            if (s >= 13) { At[2 * s - 26] = *(LAS bf16x8*)(sm + atr + ao + (2 * s - 26) * 1024); At[2 * s - 25] = *(LAS bf16x8*)(sm + atr + ao + (2 * s - 25) * 1024); }
                        }
                        SCHED();
                    }
                } else {
                    *(LAS u32x4*)(sm + atw + ao) = __builtin_bit_cast(u32x4, packstep(X, 0)) & msk[0];
                    *(LAS u32x4*)(sm + atw + ao + 1024) = __builtin_bit_cast(u32x4, packstep(X, 1)) & msk[1];
                    sig(fb, F_AT + c);
                }
                MEMFENCE();
                if (c == 0 || NOT_YET(afl, 4)) {
                    SLOW_WAIT(F_AT + c, 4);
                    UNROLL for (int i = 0; i < 6; ++i) At[i] = *(LAS bf16x8*)(sm + atr + ao + i * 1024);
                }
                MEMFENCE();
                if (c == 0) { UNROLL for (int tl = 0; tl < 2; ++tl) UNROLL for (int i = 0; i < 16; ++i) o[c][tl][i] = 0.f; }
                o[c][0] = MFMA(Vp[c][0], At[0], o[c][0]);
                o[c][0] = MFMA(Vp[c][1], At[1], o[c][0]);
                o[c][1] = MFMA(Vp[c][0], At[2], o[c][1]);
                SCHED();
                if (c < 3) XPRE((c + 1) & 3);
                SCHED();
                o[c][1] = MFMA(Vp[c][1], At[3], o[c][1]);
                o[c][1] = MFMA(Vp[c][2], At[4], o[c][1]);
                o[c][1] = MFMA(Vp[c][3], At[5], o[c][1]);
                SCHED();
                if (c < 2) VPRE((c + 2) & 3);
                if (c == 0) PUBLISH();
            }
        }
        PIN(atr);
        l16 = atr - L_AT;
        r = (l16 >> 4) & 31; h = l16 >> 9;
        const u32 ob = r * 256 + (((4 * w + h) ^ (r & 7)) << 4);
        if (j > 0) {
            bf16x8 Sin[8], Qs[64];
            LAS char* t0[2][2];
            UNROLL for (int pa = 0; pa < 2; ++pa) UNROLL for (int jj = 0; jj < 2; ++jj) { u32 a_ = (u32)(tq[pa][jj] - sm) - L_V + L_X0; PIN(a_); t0[pa][jj] = sm + a_; }
#define TRT(s) (((s) >> 4) == 2 ? TRF(tq, 32768, (s) & 7, ((s) >> 3) & 1) : TRF(t0, ((s) >> 4) * 16384, (s) & 7, ((s) >> 3) & 1))
            u32 spa = L_KT + 16384 + w * 1024 + l16, spb = L_V + 32768 + w * 1024 + l16;
            PIN(spa); PIN(spb);
#define SINP(kk) (sm + ((kk) < 4 ? spa : spb) + (kk) * 4096)
            u32 ocn = 0, qbn = 0;
            {
                MEMFENCE();
                const u32 sfl = FLAG(F_SR);
                MEMFENCE();
                UNROLL for (int kk = 0; kk < 8; ++kk) Sin[kk] = *(LAS bf16x8*)SINP(kk);
                MEMFENCE();
                UNROLL for (int s = 0; s < 3; ++s) Qs[s] = TRT(s);
                MEMFENCE();
                if (NOT_YET(sfl, 3)) {
                    SLOW_WAIT(F_SR, 3);
                    UNROLL for (int kk = 0; kk < 8; ++kk) Sin[kk] = *(LAS bf16x8*)SINP(kk);
                }
                MEMFENCE();
                UNROLL for (int s = 3; s < 5; ++s) Qs[s] = TRT(s);
            }
            u32x4 op[2];
            static_for<0, 72>([&](auto sc_) __attribute__((always_inline)) {
                constexpr int s = decltype(sc_)::value, u = s >> 3, kk = s & 7;
                if constexpr (s < 64) {
                    o[u >> 1][u & 1] = MFMA(Sin[kk], Qs[s], o[u >> 1][u & 1]);
                    SCHED();
                    if constexpr (s + 5 < 64) Qs[s + 5] = TRT(s + 5);
                }
                if (s == 36) { MEMFENCE(); ocn = FLAG(F_OC); qbn = FLAG(F_QB); }
                if (s == 40) { if (NOT_YET(ocn, 4)) SLOW_WAIT(F_OC + 0, 4); if (NOT_YET(qbn, 1)) SLOW_WAIT(F_QB, 1); MEMFENCE(); }
                if constexpr (u > 0) {
                    constexpr int p = u - 1;
                    const f32x16& x = o[p >> 1][p & 1];
                    if constexpr (kk >= 2 && kk <= 5) { constexpr int i = kk - 2; op[i >> 1][2 * (i & 1)] = pk(x[4 * i], x[4 * i + 1]); op[i >> 1][2 * (i & 1) + 1] = pk(x[4 * i + 2], x[4 * i + 3]); }
                    if constexpr (kk == 4) *(LAS u32x4*)(sm + OSLOT(p >> 1) + (p & 1) * 8192 + ob) = op[0];
                    if constexpr (kk == 7) { *(LAS u32x4*)(sm + OSLOT(p >> 1) + (p & 1) * 8192 + (ob ^ 32)) = op[1]; if (p & 1) sig(fb, F_OC + (p >> 1)); }
                }
                SCHED();
            });
        } else {
            UNROLL for (int c = 0; c < 4; ++c) {
                if (c == 2) wait_ge(fb, F_OC + 0, 4);
                UNROLL for (int x = 0; x < 4; ++x) *(LAS bf16x8*)(sm + OSLOT(c) + (x >> 1) * 8192 + (ob ^ ((x & 1) << 5))) = packstep(o[c][x >> 1], x & 1);
                sig(fb, F_OC + c);
            }
        }
        return;
        }
    }
slow_path:
    slow_segment(q_, k_, v_, g_, out_, ws_, sm, head, j);
}

__device__ __attribute__((used, noinline)) void gla_text_end() { asm volatile(".Lgla_text_end:"); }

extern "C" void kernel_launch(void* const* d_in, const int* in_sizes, int n_in, void* d_out, int out_size, void* d_ws, size_t ws_size,
                              hipStream_t stream) {
    gla_kernel<<<256, 512, 0, stream>>>((const u32*)d_in[0], (const u32*)d_in[1], (const u32*)d_in[2], (const u32*)d_in[3], (u32*)d_out, (char*)d_ws);
}
```
